```python
import torch

def _softmax(x, axis=-1):
    return torch.softmax(x, dim=axis)
def _gelu(x):
    return torch.nn.functional.gelu(x, approximate="tanh")
def _silu(x):
    return torch.nn.functional.silu(x)
def _sigmoid(x):
    return torch.sigmoid(x)
def _layernorm(x, axis=-1, eps=1e-5):
    m = x.mean(dim=axis, keepdim=True)
    v = x.var(dim=axis, keepdim=True, unbiased=False)
    return (x - m) / torch.sqrt(v + eps)
def _scatter_add(tgt, idx, src, axis=0):
    return tgt.clone().index_add_(axis, idx, src)
def _segment_sum(src, seg, n):
    out = torch.zeros((n,) + src.shape[1:], dtype=src.dtype)
    return out.index_add_(0, seg, src)
def _pad(x, pw):
    flat = tuple(v for pair in reversed(pw) for v in pair)
    return torch.nn.functional.pad(x, flat)

kernel_name = 'sequential_preLN__gqa_block_cached__dense_activation_squared_relu__decode'

def setup_inputs():

    _w = (lambda a: (lambda t: t.to(torch.bfloat16) if t.is_floating_point() else t)(torch.as_tensor(a)))
    def _A_setup():
        B, Sq, D = 1, 1, 512
        Hq, Hkv, Sk, Dh = 32, 8, 65536, 128
        assert Hq % Hkv == 0, (Hq, Hkv)
        return {
            "x": _w((lambda *s: torch.randn(*s, dtype=torch.bfloat16) * 0.05)(B, Sq, D)),
            "k_cache": _w((lambda *s: torch.randn(*s, dtype=torch.bfloat16) * 3.2)(B, Hkv, Sk, Dh)),
            "v_cache": _w((lambda *s: torch.randn(*s, dtype=torch.bfloat16) * 0.4)(B, Hkv, Sk, Dh)),
            "wq": _w((lambda *s: torch.randn(*s, dtype=torch.bfloat16) * 3.2)(D, Hq * Dh) * (D ** -0.5)),
            "wo": _w((lambda *s: torch.randn(*s, dtype=torch.bfloat16) * 0.4)(Hq * Dh, D) * ((Hq * Dh) ** -0.5)),
        }
    _A_in = _A_setup()

    def _B_setup():
        B, S, D, F = 1, 1, 512, 1792
        return {
            "x": _w((lambda *s: torch.randn(*s, dtype=torch.bfloat16) * 0.05)(B, S, D)),
            "w1": _w((lambda *s: torch.randn(*s, dtype=torch.bfloat16) * 0.05)(D, F)),
            "b1": _w((lambda *s: torch.randn(*s, dtype=torch.bfloat16) * 0.8)(F)),
            "w2": _w((lambda *s: torch.randn(*s, dtype=torch.bfloat16) * 0.05)(F, D)),
            "b2": _w((lambda *s: torch.randn(*s, dtype=torch.bfloat16) * 3.2)(D)),
        }
    _B_in = _B_setup()

    return {
        "A_x": _A_in["x"],
        "A_k_cache": _A_in["k_cache"],
        "A_v_cache": _A_in["v_cache"],
        "A_wq": _A_in["wq"],
        "A_wo": _A_in["wo"],
        "B_x": _B_in["x"],
        "B_w1": _B_in["w1"],
        "B_b1": _B_in["b1"],
        "B_w2": _B_in["w2"],
        "B_b2": _B_in["b2"],
    }

def reference(A_x, A_k_cache, A_v_cache, A_wq, A_wo, B_x, B_w1, B_b1, B_w2, B_b2):
    def _A_ref(x, k_cache, v_cache, wq, wo):
        B, Sq, D = x.shape
        Hq, Dh = 32, 128
        Hkv = k_cache.shape[1]
        rep = Hq // Hkv
        q = (lambda x, *d: x.permute(*d))((x @ wq).reshape(B, Sq, Hq, Dh), 0, 2, 1, 3)
        k = (lambda x, n, axis: torch.repeat_interleave(x, n, dim=axis))(k_cache, rep, axis=1)
        v = (lambda x, n, axis: torch.repeat_interleave(x, n, dim=axis))(v_cache, rep, axis=1)
        scores = torch.einsum("bhqd,bhkd->bhqk", q, k) * (Dh ** -0.5)
        p = _softmax(scores, axis=-1)
        o = torch.einsum("bhqk,bhkd->bhqd", p, v)
        return (lambda x, *d: x.permute(*d))(o, 0, 2, 1, 3).reshape(B, Sq, Hq * Dh) @ wo

    def _B_ref(x, w1, b1, w2, b2):
        h = torch.einsum("bsd,df->bsf", x, w1) + b1
        zero = torch.zeros_like(h)
        r = torch.where(h > zero, h, zero)
        h = r * r
        out = torch.einsum("bsf,fd->bsd", h, w2) + b2
        return out

    x = A_x
    h = x + _A_ref(_layernorm(x, axis=-1), A_k_cache, A_v_cache, A_wq, A_wo)
    h2 = h + _B_ref(_layernorm(h, axis=-1), B_w1, B_b1, B_w2, B_b2)
    return h2
```

```cpp
#include <hip/hip_runtime.h>
#include <hip/hip_bf16.h>
#include <cstdint>

typedef unsigned short bf16_t;
using bf16x2 = __attribute__((ext_vector_type(2))) __bf16;
using u32x4  = __attribute__((ext_vector_type(4))) unsigned;
using f32x4  = __attribute__((ext_vector_type(4))) float;

constexpr int DM = 512, HQ = 32, HKV = 8, SK = 65536, DH = 128, FF = 1792;
constexpr int NSPLIT = 32, KEYS_PER_WG = SK / NSPLIT, PAGE = 16, WAVES = 8, NTHR = WAVES * 64;
constexpr int PAGES_PER_WAVE = KEYS_PER_WG / PAGE / WAVES;
constexpr int NB = 4;
constexpr float SCALE = 0.08838834764831845f;
constexpr float LOG2E = 1.4426950408889634f;

constexpr size_t WS_Q = 0;
constexpr size_t WS_PML = 16384;
constexpr size_t WS_PO = WS_PML + 32 * 32 * 2 * 4;
constexpr size_t WS_O = WS_PO + 32 * 32 * 128 * 4;
constexpr size_t WS_H = WS_O + 4096 * 4;
constexpr size_t WS_U = WS_H + 512 * 4;

__device__ __forceinline__ float bf2f(unsigned b) { return __builtin_bit_cast(float, b << 16); }
__device__ __forceinline__ float bfr(float x) { return (float)(__bf16)x; }
__device__ __forceinline__ unsigned f2bf(float x) { return (unsigned)__builtin_bit_cast(unsigned short, (__bf16)x); }
__device__ __forceinline__ float dot2(unsigned k, unsigned q, float acc) {
  return __builtin_amdgcn_fdot2_f32_bf16(__builtin_bit_cast(bf16x2, k), __builtin_bit_cast(bf16x2, q), acc, false);
}
template <int CTRL> __device__ __forceinline__ float dpp(float x) {
  return __builtin_bit_cast(float, __builtin_amdgcn_mov_dpp(__builtin_bit_cast(int, x), CTRL, 0xf, 0xf, true));
}
constexpr int XOR1 = 0xB1, XOR2 = 0x4E, XOR7 = 0x141, XOR8 = 0x128, ROR4 = 0x124;
__device__ __forceinline__ float xrow16_max(float x) {
  auto s = __builtin_amdgcn_permlane16_swap(__float_as_uint(x), __float_as_uint(x), false, false);
  x = fmaxf(__uint_as_float(s[0]), __uint_as_float(s[1]));
  auto t = __builtin_amdgcn_permlane32_swap(__float_as_uint(x), __float_as_uint(x), false, false);
  return fmaxf(__uint_as_float(t[0]), __uint_as_float(t[1]));
}
__device__ __forceinline__ float xrow16_sum(float x) {
  auto s = __builtin_amdgcn_permlane16_swap(__float_as_uint(x), __float_as_uint(x), false, false);
  x = __uint_as_float(s[0]) + __uint_as_float(s[1]);
  auto t = __builtin_amdgcn_permlane32_swap(__float_as_uint(x), __float_as_uint(x), false, false);
  return __uint_as_float(t[0]) + __uint_as_float(t[1]);
}
__device__ __forceinline__ float wave_sum(float x) {
  x += dpp<XOR1>(x); x += dpp<XOR2>(x); x += dpp<XOR7>(x); x += dpp<XOR8>(x);
  return xrow16_sum(x);
}

__device__ __forceinline__ void ln_stats(const float* v, float* red, float& m_bf, float& d_bf) {
  const int tid = threadIdx.x, lane = tid & 63;
  float s = 0.f;
  for (int i = lane; i < DM; i += 64) s += v[i];
  s = wave_sum(s);
  const float mean = s * (1.f / DM);
  float q = 0.f;
  for (int i = lane; i < DM; i += 64) { const float d = v[i] - mean; q = fmaf(d, d, q); }
  q = wave_sum(q);
  const float var = bfr(q * (1.f / DM));
  m_bf = bfr(mean);
  d_bf = bfr(sqrtf(bfr(var + 1e-5f)));
}

__global__ void __launch_bounds__(256) k_qproj(const bf16_t* __restrict__ x, const bf16_t* __restrict__ wq, bf16_t* __restrict__ q) {
  __shared__ float xs[DM];
  __shared__ float red[32][64 + 1];
  const int tid = threadIdx.x;
  for (int i = tid; i < DM; i += 256) xs[i] = bf2f(x[i]);
  __syncthreads();
  float m, d; ln_stats(xs, nullptr, m, d);
  const int cg = tid & 7, rg = tid >> 3, col0 = blockIdx.x * 64 + cg * 8;
  float acc[8] = {0, 0, 0, 0, 0, 0, 0, 0};
#pragma unroll
  for (int i = 0; i < 16; ++i) {
    const int row = rg + 32 * i;
    const u32x4 w = *reinterpret_cast<const u32x4*>(wq + (size_t)row * (HQ * DH) + col0);
    const float xn = bfr(bfr(xs[row] - m) / d);
#pragma unroll
    for (int k = 0; k < 4; ++k) { acc[2 * k] = fmaf(xn, bf2f(w[k] & 0xffffu), acc[2 * k]); acc[2 * k + 1] = fmaf(xn, bf2f(w[k] >> 16), acc[2 * k + 1]); }
  }
#pragma unroll
  for (int k = 0; k < 8; ++k) red[rg][cg * 8 + k] = acc[k];
  __syncthreads();
  if (tid < 64) { float s = 0.f;
    for (int r = 0; r < 32; ++r) s += red[r][tid];
    q[blockIdx.x * 64 + tid] = (bf16_t)f2bf(s); }
}

__global__ void __launch_bounds__(NTHR, 2) k_attn(const bf16_t* __restrict__ kc, const bf16_t* __restrict__ vc, const bf16_t* __restrict__ q,
                                                  float* __restrict__ pml, float* __restrict__ po) {
  __shared__ __attribute__((aligned(16))) float mrg[WAVES][4][4 + DH];
  const int g = blockIdx.x & 7, s = blockIdx.x >> 3;
  const int tid = threadIdx.x, lane = tid & 63, wave = __builtin_amdgcn_readfirstlane(tid >> 6), r = lane >> 4, c = lane & 15;
  const size_t base = ((size_t)g * SK + (size_t)s * KEYS_PER_WG + (size_t)wave * PAGE + r) * DH + 8 * c;
  const bf16_t* kp = kc + base;
  const bf16_t* vp = vc + base;
  u32x4 Kb[NB][4], Vb[NB][4];
#define ISSUE(b_, i_) do { _Pragma("unroll") for (int t_ = 0; t_ < 4; ++t_) { \
    Kb[b_][t_] = __builtin_nontemporal_load(reinterpret_cast<const u32x4*>(kp + (size_t)(i_) * (WAVES * PAGE * DH) + t_ * 4 * DH)); \
    Vb[b_][t_] = __builtin_nontemporal_load(reinterpret_cast<const u32x4*>(vp + (size_t)(i_) * (WAVES * PAGE * DH) + t_ * 4 * DH)); } } while (0)
  u32x4 qpk[4];
#pragma unroll
  for (int h = 0; h < 4; ++h) qpk[h] = *reinterpret_cast<const u32x4*>(q + (g * 4 + h) * DH + 8 * c);
#pragma unroll
  for (int b = 0; b < NB; ++b) ISSUE(b, b);
  float m = -INFINITY, l = 0.f, acc[32];
#pragma unroll
  for (int k = 0; k < 32; ++k) acc[k] = 0.f;
  for (int i0 = 0; i0 < PAGES_PER_WAVE; i0 += NB) {
#pragma unroll
    for (int b = 0; b < NB; ++b) {
      float x[16];
#pragma unroll
      for (int t = 0; t < 4; ++t)
#pragma unroll
        for (int h = 0; h < 4; ++h) { float d = 0.f;
#pragma unroll
          for (int k = 0; k < 4; ++k) d = dot2(Kb[b][t][k], qpk[h][k], d);
          x[t * 4 + h] = d; }
#define TR_STEP(HALF, CTRL, BIT) _Pragma("unroll") for (int hh = 0; hh < (HALF); ++hh) { \
      const float lo_ = x[hh], hi_ = x[hh + (HALF)]; const float t1_ = lo_ + dpp<CTRL>(lo_), t2_ = hi_ + dpp<CTRL>(hi_); \
      x[hh] = (c & (BIT)) ? t2_ : t1_; }
      TR_STEP(8, XOR8, 8) TR_STEP(4, XOR7, 4) TR_STEP(2, XOR2, 2) TR_STEP(1, XOR1, 1)
#undef TR_STEP
      const float sc = bfr(bfr(x[0]) * SCALE);
      float pm = sc; pm = fmaxf(pm, dpp<ROR4>(pm)); pm = fmaxf(pm, dpp<XOR8>(pm)); pm = xrow16_max(pm);
      if (__any(pm > m)) {
        const float mn = fmaxf(m, pm);
        const float alpha = __builtin_amdgcn_exp2f((m - mn) * LOG2E);
        l *= alpha; m = mn;
        const float a0 = dpp<0x00>(alpha), a1 = dpp<0x55>(alpha), a2 = dpp<0xAA>(alpha), a3 = dpp<0xFF>(alpha);
#pragma unroll
        for (int k = 0; k < 8; ++k) { acc[k] *= a0; acc[8 + k] *= a1; acc[16 + k] *= a2; acc[24 + k] *= a3; }
      }
      const float p = __builtin_amdgcn_exp2f((sc - m) * LOG2E);
      l += p;
#pragma unroll
      for (int t = 0; t < 4; ++t) {
        float vf[8];
#pragma unroll
        for (int k = 0; k < 4; ++k) { vf[2 * k] = __builtin_bit_cast(float, Vb[b][t][k] << 16); vf[2 * k + 1] = __builtin_bit_cast(float, Vb[b][t][k] & 0xffff0000u); }
        float pb[4];
        if (t == 0) { pb[0] = dpp<0x150>(p); pb[1] = dpp<0x151>(p); pb[2] = dpp<0x152>(p); pb[3] = dpp<0x153>(p); }
        if (t == 1) { pb[0] = dpp<0x154>(p); pb[1] = dpp<0x155>(p); pb[2] = dpp<0x156>(p); pb[3] = dpp<0x157>(p); }
        if (t == 2) { pb[0] = dpp<0x158>(p); pb[1] = dpp<0x159>(p); pb[2] = dpp<0x15A>(p); pb[3] = dpp<0x15B>(p); }
        if (t == 3) { pb[0] = dpp<0x15C>(p); pb[1] = dpp<0x15D>(p); pb[2] = dpp<0x15E>(p); pb[3] = dpp<0x15F>(p); }
#pragma unroll
        for (int h = 0; h < 4; ++h)
#pragma unroll
          for (int k = 0; k < 8; ++k) acc[h * 8 + k] = fmaf(pb[h], vf[k], acc[h * 8 + k]);
      }
      if (i0 + NB < PAGES_PER_WAVE) ISSUE(b, i0 + b + NB);
    }
  }
#undef ISSUE
#pragma unroll
  for (int k = 0; k < 32; ++k) acc[k] = xrow16_sum(acc[k]);
  { float t = l; t += dpp<ROR4>(t); t += dpp<XOR8>(t); l = xrow16_sum(t); }
  if (r == 0) {
#pragma unroll
    for (int h = 0; h < 4; ++h) {
      float* rec = &mrg[wave][h][0];
      *reinterpret_cast<f32x4*>(rec + 4 + 8 * c) = f32x4{acc[h * 8], acc[h * 8 + 1], acc[h * 8 + 2], acc[h * 8 + 3]};
      *reinterpret_cast<f32x4*>(rec + 8 + 8 * c) = f32x4{acc[h * 8 + 4], acc[h * 8 + 5], acc[h * 8 + 6], acc[h * 8 + 7]};
      if (c == h) { rec[0] = m; rec[1] = l; }
    }
  }
  __syncthreads();
  {
    const int h = tid >> 7, d = tid & 127;
    float M = -INFINITY;
#pragma unroll
    for (int w = 0; w < WAVES; ++w) M = fmaxf(M, mrg[w][h][0]);
    float L = 0.f, o = 0.f;
#pragma unroll
    for (int w = 0; w < WAVES; ++w) { const float wt = __builtin_amdgcn_exp2f((mrg[w][h][0] - M) * LOG2E);
      L = fmaf(wt, mrg[w][h][1], L); o = fmaf(wt, mrg[w][h][4 + d], o); }
    const int slot = (g * 4 + h) * NSPLIT + s;
    po[(size_t)slot * DH + d] = o;
    if (d == 0) { pml[slot * 2] = M; pml[slot * 2 + 1] = L; }
  }
}

__global__ void __launch_bounds__(128) k_comb(const float* __restrict__ pml, const float* __restrict__ po, float* __restrict__ o) {
  const int h = blockIdx.x, d = threadIdx.x;
  float M = -INFINITY;
  for (int s = 0; s < NSPLIT; ++s) M = fmaxf(M, pml[(h * NSPLIT + s) * 2]);
  float L = 0.f, a = 0.f;
  for (int s = 0; s < NSPLIT; ++s) { const float wt = __builtin_amdgcn_exp2f((pml[(h * NSPLIT + s) * 2] - M) * LOG2E);
    L = fmaf(wt, pml[(h * NSPLIT + s) * 2 + 1], L); a = fmaf(wt, po[(size_t)(h * NSPLIT + s) * DH + d], a); }
  o[h * DH + d] = bfr(a / L);
}

__device__ __forceinline__ void block_reduce8(float (&acc)[8], float (*red)[8], int nw) {
  const int lane = threadIdx.x & 63, wave = threadIdx.x >> 6;
#pragma unroll
  for (int k = 0; k < 8; ++k) acc[k] = wave_sum(acc[k]);
  if (lane == 0) {
#pragma unroll
    for (int k = 0; k < 8; ++k) red[wave][k] = acc[k]; }
  __syncthreads();
  if (threadIdx.x < 8) { float s = 0.f; for (int w = 0; w < nw; ++w) s += red[w][threadIdx.x]; red[0][threadIdx.x] = s; }
}

__global__ void __launch_bounds__(256) k_oproj(const bf16_t* __restrict__ x, const bf16_t* __restrict__ wo, const float* __restrict__ o, float* __restrict__ hbuf) {
  __shared__ float red[4][8];
  const int tid = threadIdx.x, col0 = blockIdx.x * 8;
  float acc[8] = {0, 0, 0, 0, 0, 0, 0, 0};
#pragma unroll
  for (int i = 0; i < 16; ++i) {
    const int row = tid + 256 * i;
    const u32x4 w = *reinterpret_cast<const u32x4*>(wo + (size_t)row * DM + col0);
    const float ov = o[row];
#pragma unroll
    for (int k = 0; k < 4; ++k) { acc[2 * k] = fmaf(ov, bf2f(w[k] & 0xffffu), acc[2 * k]); acc[2 * k + 1] = fmaf(ov, bf2f(w[k] >> 16), acc[2 * k + 1]); }
  }
  block_reduce8(acc, red, 4);
  if (tid < 8) hbuf[col0 + tid] = bfr(bf2f(x[col0 + tid]) + bfr(red[0][tid]));
}

__global__ void __launch_bounds__(256) k_up(const float* __restrict__ hbuf, const bf16_t* __restrict__ w1, const bf16_t* __restrict__ b1, float* __restrict__ u) {
  __shared__ float hs[DM];
  __shared__ float red[4][8];
  const int tid = threadIdx.x, col0 = blockIdx.x * 8;
  for (int i = tid; i < DM; i += 256) hs[i] = hbuf[i];
  __syncthreads();
  float m, d; ln_stats(hs, nullptr, m, d);
  float acc[8] = {0, 0, 0, 0, 0, 0, 0, 0};
#pragma unroll
  for (int i = 0; i < 2; ++i) {
    const int row = tid + 256 * i;
    const u32x4 w = *reinterpret_cast<const u32x4*>(w1 + (size_t)row * FF + col0);
    const float hn = bfr(bfr(hs[row] - m) / d);
#pragma unroll
    for (int k = 0; k < 4; ++k) { acc[2 * k] = fmaf(hn, bf2f(w[k] & 0xffffu), acc[2 * k]); acc[2 * k + 1] = fmaf(hn, bf2f(w[k] >> 16), acc[2 * k + 1]); }
  }
  block_reduce8(acc, red, 4);
  if (tid < 8) { const float hv = bfr(bfr(red[0][tid]) + bf2f(b1[col0 + tid])); const float rr = hv > 0.f ? hv : 0.f; u[col0 + tid] = bfr(rr * rr); }
}

__global__ void __launch_bounds__(256) k_down(const float* __restrict__ hbuf, const float* __restrict__ u, const bf16_t* __restrict__ w2, const bf16_t* __restrict__ b2, bf16_t* __restrict__ out) {
  __shared__ float red[4][8];
  const int tid = threadIdx.x, col0 = blockIdx.x * 8;
  float acc[8] = {0, 0, 0, 0, 0, 0, 0, 0};
#pragma unroll
  for (int i = 0; i < 7; ++i) {
    const int row = tid + 256 * i;
    const u32x4 w = *reinterpret_cast<const u32x4*>(w2 + (size_t)row * DM + col0);
    const float uv = u[row];
#pragma unroll
    for (int k = 0; k < 4; ++k) { acc[2 * k] = fmaf(uv, bf2f(w[k] & 0xffffu), acc[2 * k]); acc[2 * k + 1] = fmaf(uv, bf2f(w[k] >> 16), acc[2 * k + 1]); }
  }
  block_reduce8(acc, red, 4);
  if (tid < 8) { const float ov = bfr(bfr(red[0][tid]) + bf2f(b2[col0 + tid])); out[col0 + tid] = (bf16_t)f2bf(hbuf[col0 + tid] + ov); }
}

extern "C" void kernel_launch(void* const* d_in, const int* in_sizes, int n_in,
                              void* d_out, int out_size, void* d_ws, size_t ws_size,
                              hipStream_t stream) {
  const bf16_t* x  = (const bf16_t*)d_in[0];
  const bf16_t* kc = (const bf16_t*)d_in[1];
  const bf16_t* vc = (const bf16_t*)d_in[2];
  const bf16_t* wq = (const bf16_t*)d_in[3];
  const bf16_t* wo = (const bf16_t*)d_in[4];
  const bf16_t* w1 = (const bf16_t*)d_in[6];
  const bf16_t* b1 = (const bf16_t*)d_in[7];
  const bf16_t* w2 = (const bf16_t*)d_in[8];
  const bf16_t* b2 = (const bf16_t*)d_in[9];
  char* ws = (char*)d_ws;
  bf16_t* q = (bf16_t*)(ws + WS_Q);
  float* pml = (float*)(ws + WS_PML);
  float* po = (float*)(ws + WS_PO);
  float* o = (float*)(ws + WS_O);
  float* hbuf = (float*)(ws + WS_H);
  float* u = (float*)(ws + WS_U);
  k_qproj<<<64, 256, 0, stream>>>(x, wq, q);
  k_attn<<<HKV * NSPLIT, NTHR, 0, stream>>>(kc, vc, q, pml, po);
  k_comb<<<HQ, 128, 0, stream>>>(pml, po, o);
  k_oproj<<<DM / 8, 256, 0, stream>>>(x, wo, o, hbuf);
  k_up<<<FF / 8, 256, 0, stream>>>(hbuf, w1, b1, u);
  k_down<<<DM / 8, 256, 0, stream>>>(hbuf, u, w2, b2, (bf16_t*)d_out);
}
```

```cpp
#include <hip/hip_runtime.h>
#include <hip/hip_bf16.h>
#include <cstdint>

typedef unsigned short bf16_t;
using bf16x2 = __attribute__((ext_vector_type(2))) __bf16;
using u32x4  = __attribute__((ext_vector_type(4))) unsigned;
using u32x2  = __attribute__((ext_vector_type(2))) unsigned;
using f32x4  = __attribute__((ext_vector_type(4))) float;
#define LAS __attribute__((address_space(3)))

constexpr int DM = 512, HQ = 32, HKV = 8, SK = 65536, DH = 128, FF = 1792;
constexpr int NSPLIT = 32, NWG = HKV * NSPLIT, KEYS_PER_WG = SK / NSPLIT, PAGE = 16, WAVES = 8, NTHR = WAVES * 64;
constexpr int PPW = KEYS_PER_WG / PAGE / WAVES;
#define NBUF 3
constexpr int NB = NBUF;
constexpr int NUP = FF / 8, NDOWN = DM / 8;
constexpr float SCALE = 0.08838834764831845f;
constexpr float LOG2E = 1.4426950408889634f;
constexpr int SPIN_MAX = 1 << 18;

constexpr unsigned WS_EPOCH = 0;
constexpr unsigned WS_QG = 256;
constexpr unsigned WS_PA = WS_QG + 8 * 32 * 4 * 16;
constexpr unsigned WS_AP = WS_PA + 8 * 32 * 32 * 9 * 16;
constexpr unsigned WS_HG = WS_AP + 256 * 256 * 16;
constexpr unsigned WS_UG = WS_HG + 256 * 16;
constexpr unsigned WS_END = WS_UG + 224 * 2 * 16;

constexpr int L_WO = 0;
constexpr int L_W1 = L_WO + 16384;
constexpr int L_W2 = L_W1 + 8192;
constexpr int L_MRG = L_W2 + 28672;
constexpr int L_Q = L_MRG + 8 * 4 * 132 * 4;
constexpr int L_RED = L_Q + 1024;
constexpr int L_H = L_RED + 512;
constexpr int L_U = L_H + 2048;
constexpr int L_IN = L_U + 7168;
constexpr int L_END = L_IN + 32 * 18 * 4;

__device__ __forceinline__ float bf2f(unsigned b) { return __builtin_bit_cast(float, b << 16); }
__device__ __forceinline__ float bfhi(unsigned b) { return __builtin_bit_cast(float, b & 0xffff0000u); }
__device__ __forceinline__ float bfr(float x) { return (float)(__bf16)x; }
__device__ __forceinline__ unsigned f2bf(float x) { return (unsigned)__builtin_bit_cast(unsigned short, (__bf16)x); }
__device__ __forceinline__ float u2f(unsigned u) { return __builtin_bit_cast(float, u); }
__device__ __forceinline__ unsigned f2u(float f) { return __builtin_bit_cast(unsigned, f); }
__device__ __forceinline__ float dot2(unsigned k, unsigned q, float acc) {
  return __builtin_amdgcn_fdot2_f32_bf16(__builtin_bit_cast(bf16x2, k), __builtin_bit_cast(bf16x2, q), acc, false);
}
template <int CTRL> __device__ __forceinline__ float dpp(float x) {
  return __builtin_bit_cast(float, __builtin_amdgcn_mov_dpp(__builtin_bit_cast(int, x), CTRL, 0xf, 0xf, true));
}
constexpr int XOR1 = 0xB1, XOR2 = 0x4E, XOR7 = 0x141, XOR8 = 0x128, ROR4 = 0x124;
__device__ __forceinline__ float xrow16_max(float x) {
  auto s = __builtin_amdgcn_permlane16_swap(__float_as_uint(x), __float_as_uint(x), false, false);
  x = fmaxf(__uint_as_float(s[0]), __uint_as_float(s[1]));
  auto t = __builtin_amdgcn_permlane32_swap(__float_as_uint(x), __float_as_uint(x), false, false);
  return fmaxf(__uint_as_float(t[0]), __uint_as_float(t[1]));
}
__device__ __forceinline__ float xrow16_sum(float x) {
  auto s = __builtin_amdgcn_permlane16_swap(__float_as_uint(x), __float_as_uint(x), false, false);
  x = __uint_as_float(s[0]) + __uint_as_float(s[1]);
  auto t = __builtin_amdgcn_permlane32_swap(__float_as_uint(x), __float_as_uint(x), false, false);
  return __uint_as_float(t[0]) + __uint_as_float(t[1]);
}
__device__ __forceinline__ float wave_sum(float x) {
  x += dpp<XOR1>(x); x += dpp<XOR2>(x); x += dpp<XOR7>(x); x += dpp<XOR8>(x);
  return xrow16_sum(x);
}
__device__ __forceinline__ float wave_max(float x) {
  x = fmaxf(x, dpp<XOR1>(x)); x = fmaxf(x, dpp<XOR2>(x)); x = fmaxf(x, dpp<XOR7>(x)); x = fmaxf(x, dpp<XOR8>(x));
  return xrow16_max(x);
}
__device__ __forceinline__ void ln_stats8(const float (&v)[8], float& m_bf, float& d_bf) {
  float s = 0.f;
#pragma unroll
  for (int k = 0; k < 8; ++k) s += v[k];
  const float mean = wave_sum(s) * (1.f / DM);
  float q = 0.f;
#pragma unroll
  for (int k = 0; k < 8; ++k) { const float d = v[k] - mean; q = fmaf(d, d, q); }
  const float var = bfr(wave_sum(q) * (1.f / DM));
  m_bf = bfr(mean);
  d_bf = bfr(sqrtf(bfr(var + 1e-5f)));
}
__device__ __forceinline__ void glds16(const void* gsrc, unsigned lds_dst) {
  unsigned keep;
  asm volatile("s_mov_b32 %0, m0\n\ts_mov_b32 m0, %2\n\ts_nop 0\n\tglobal_load_lds_dwordx4 %1, off\n\ts_mov_b32 m0, %0"
               : "=&s"(keep) : "v"(gsrc), "s"(lds_dst) : "memory");
}

struct Params { const bf16_t *x, *kc, *vc, *wq, *wo, *w1, *b1, *w2, *b2; bf16_t* out; unsigned char* ws; };

#define GSTORE(off_, d0_, d1_) __builtin_amdgcn_raw_buffer_store_b128(u32x4{tag, (d0_), tag, (d1_)}, rsrc, (off_), 0, 16)
#define GLOAD(off_) __builtin_amdgcn_raw_buffer_load_b128(rsrc, (off_), 0, 16)

__global__ void __launch_bounds__(NTHR, 2) fused_decode(Params p) {
  __shared__ __attribute__((aligned(16))) unsigned char smem[L_END];
  const int wg = blockIdx.x, g = wg & 7, s = wg >> 3;
  const int tid = threadIdx.x, lane = tid & 63, wave = __builtin_amdgcn_readfirstlane(tid >> 6), r = lane >> 4, c = lane & 15;
  const auto rsrc = __builtin_amdgcn_make_buffer_rsrc(p.ws, 0, WS_END, 0x00020000);
  float* const red = reinterpret_cast<float*>(smem + L_RED);

  unsigned ev = __hip_atomic_load(reinterpret_cast<unsigned*>(p.ws + WS_EPOCH), __ATOMIC_RELAXED, __HIP_MEMORY_SCOPE_AGENT);
  u32x4 xv = *reinterpret_cast<const u32x4*>(p.x + 8 * lane);
  unsigned xt = p.x[tid];
  const bf16_t* wqp = p.wq + (size_t)tid * (HQ * DH) + g * 512 + s * 16;
  u32x4 wq0 = *reinterpret_cast<const u32x4*>(wqp), wq1 = *reinterpret_cast<const u32x4*>(wqp + 8);

  constexpr int SW = WAVES - 1;
  constexpr int NPG = KEYS_PER_WG / PAGE;
  constexpr int PFULL = NPG / SW;
  float* const mrg = reinterpret_cast<float*>(smem + L_MRG);
#define TR_STEP(x, HALF, CTRL, BIT) _Pragma("unroll") for (int hh = 0; hh < (HALF); ++hh) { \
      const float lo_ = x[hh], hi_ = x[hh + (HALF)]; const float t1_ = lo_ + dpp<CTRL>(lo_), t2_ = hi_ + dpp<CTRL>(hi_); \
      x[hh] = (c & (BIT)) ? t2_ : t1_; }
  auto qpartial = [&]() {
    float xf[8];
#pragma unroll
    for (int k = 0; k < 4; ++k) { xf[2 * k] = bf2f(xv[k] & 0xffffu); xf[2 * k + 1] = bfhi(xv[k]); }
    float m, d; ln_stats8(xf, m, d);
    const float xn = bfr(bfr(bf2f(xt) - m) / d);
    float y[16];
#pragma unroll
    for (int k = 0; k < 4; ++k) { y[2 * k] = xn * bf2f(wq0[k] & 0xffffu); y[2 * k + 1] = xn * bfhi(wq0[k]);
                                  y[8 + 2 * k] = xn * bf2f(wq1[k] & 0xffffu); y[9 + 2 * k] = xn * bfhi(wq1[k]); }
    TR_STEP(y, 8, XOR8, 8) TR_STEP(y, 4, XOR7, 4) TR_STEP(y, 2, XOR2, 2) TR_STEP(y, 1, XOR1, 1)
    const float t = xrow16_sum(y[0]);
    if (r == 0) red[wave * 16 + c] = t;
  };
  unsigned tag;
  if (wave == 0) {
    asm volatile("; service wave" : "+v"(ev), "+v"(xv), "+v"(xt), "+v"(wq0), "+v"(wq1) :: "memory");
    qpartial();
    tag = __builtin_amdgcn_readfirstlane(ev) + 1u;
    __syncthreads();
    float qs = 0.f;
#pragma unroll
    for (int w = 0; w < WAVES; ++w) qs += red[w * 16 + c];
    const unsigned qb = f2bf(qs);
    const unsigned b0 = __shfl(qb, 4 * (lane & 3)), b1 = __shfl(qb, 4 * (lane & 3) + 1), b2 = __shfl(qb, 4 * (lane & 3) + 2), b3 = __shfl(qb, 4 * (lane & 3) + 3);
    if (lane < 4) GSTORE(WS_QG + ((g * 32 + s) * 4 + lane) * 16, b0 | (b1 << 16), b2 | (b3 << 16));
    u32x4 a0, a1;
    for (int spin = 0; spin < SPIN_MAX; ++spin) {
      asm volatile("" ::: "memory");
      a0 = GLOAD(WS_QG + g * 2048 + lane * 16); a1 = GLOAD(WS_QG + g * 2048 + 1024 + lane * 16);
      const bool ok = a0[0] == tag && a0[2] == tag && a1[0] == tag && a1[2] == tag;
      if (__all(ok)) break;
      __builtin_amdgcn_s_sleep(1);
    }
    *reinterpret_cast<u32x2*>(smem + L_Q + lane * 8) = u32x2{a0[1], a0[3]};
    *reinterpret_cast<u32x2*>(smem + L_Q + 512 + lane * 8) = u32x2{a1[1], a1[3]};
    __syncthreads();
    const unsigned lbase = (unsigned)(uintptr_t)(LAS unsigned char*)smem;
    const bf16_t* wop = p.wo + (size_t)(g * 512 + s * 16) * DM + 8 * lane;
#pragma unroll
    for (int i = 0; i < 16; ++i) glds16(wop + i * DM, lbase + L_WO + i * 1024);
    if (wg < NUP) {
      const bf16_t* w1p = p.w1 + (size_t)lane * FF + (g * 28 + s) * 8;
#pragma unroll
      for (int i = 0; i < 8; ++i) glds16(w1p + (size_t)i * 64 * FF, lbase + L_W1 + i * 1024);
    }
    if (wg < NDOWN) {
      const bf16_t* w2p = p.w2 + (size_t)lane * DM + (g * 8 + s) * 8;
#pragma unroll
      for (int i = 0; i < 28; ++i) glds16(w2p + (size_t)i * 64 * DM, lbase + L_W2 + i * 1024);
    }
    asm volatile("s_waitcnt vmcnt(0)" ::: "memory");
    __syncthreads();
  } else {
    const size_t base = ((size_t)g * SK + (size_t)s * KEYS_PER_WG + (size_t)(wave - 1) * PAGE + r) * DH + 8 * c;
    const bf16_t* kp = p.kc + base;
    const bf16_t* vp = p.vc + base;
    u32x4 Kb[NB][4], Vb[NB][4];
#define ISSUE(b_, i_) do { _Pragma("unroll") for (int t_ = 0; t_ < 4; ++t_) { \
    Kb[b_][t_] = __builtin_nontemporal_load(reinterpret_cast<const u32x4*>(kp + (size_t)(i_) * (SW * PAGE * DH) + t_ * 4 * DH)); \
    Vb[b_][t_] = __builtin_nontemporal_load(reinterpret_cast<const u32x4*>(vp + (size_t)(i_) * (SW * PAGE * DH) + t_ * 4 * DH)); } } while (0)
#pragma unroll
    for (int b = 0; b < NB; ++b) ISSUE(b, b);
    __builtin_amdgcn_sched_barrier(0);
    asm volatile("; streaming waves" : "+v"(ev), "+v"(xv), "+v"(xt), "+v"(wq0), "+v"(wq1) :: "memory");
    qpartial();
    tag = __builtin_amdgcn_readfirstlane(ev) + 1u;
    __syncthreads();
    __syncthreads();
    u32x4 qpk[4];
#pragma unroll
    for (int h = 0; h < 4; ++h) qpk[h] = *reinterpret_cast<const u32x4*>(smem + L_Q + h * 256 + 16 * c);
    float m = -INFINITY, l = 0.f, acc[32];
#pragma unroll
    for (int k = 0; k < 32; ++k) acc[k] = 0.f;
#define PAGE_BODY(b) do { \
      float x[16]; \
      _Pragma("unroll") for (int t = 0; t < 4; ++t) \
        _Pragma("unroll") for (int h = 0; h < 4; ++h) { float d = 0.f; \
          _Pragma("unroll") for (int k = 0; k < 4; ++k) d = dot2(Kb[b][t][k], qpk[h][k], d); \
          x[t * 4 + h] = d; } \
      TR_STEP(x, 8, XOR8, 8) TR_STEP(x, 4, XOR7, 4) TR_STEP(x, 2, XOR2, 2) TR_STEP(x, 1, XOR1, 1) \
      const float sc = bfr(bfr(x[0]) * SCALE); \
      float pm = sc; pm = fmaxf(pm, dpp<ROR4>(pm)); pm = fmaxf(pm, dpp<XOR8>(pm)); pm = xrow16_max(pm); \
      if (__any(pm > m)) { \
        const float mn = fmaxf(m, pm); \
        const float alpha = __builtin_amdgcn_exp2f((m - mn) * LOG2E); \
        l *= alpha; m = mn; \
        const float a0 = dpp<0x00>(alpha), a1 = dpp<0x55>(alpha), a2 = dpp<0xAA>(alpha), a3 = dpp<0xFF>(alpha); \
        _Pragma("unroll") for (int k = 0; k < 8; ++k) { acc[k] *= a0; acc[8 + k] *= a1; acc[16 + k] *= a2; acc[24 + k] *= a3; } \
      } \
      const float pp = __builtin_amdgcn_exp2f((sc - m) * LOG2E); \
      l += pp; \
      _Pragma("unroll") for (int t = 0; t < 4; ++t) { \
        float vf[8]; \
        _Pragma("unroll") for (int k = 0; k < 4; ++k) { vf[2 * k] = bf2f(Vb[b][t][k]); vf[2 * k + 1] = bfhi(Vb[b][t][k]); } \
        float pb[4]; \
        if (t == 0) { pb[0] = dpp<0x150>(pp); pb[1] = dpp<0x151>(pp); pb[2] = dpp<0x152>(pp); pb[3] = dpp<0x153>(pp); } \
        if (t == 1) { pb[0] = dpp<0x154>(pp); pb[1] = dpp<0x155>(pp); pb[2] = dpp<0x156>(pp); pb[3] = dpp<0x157>(pp); } \
        if (t == 2) { pb[0] = dpp<0x158>(pp); pb[1] = dpp<0x159>(pp); pb[2] = dpp<0x15A>(pp); pb[3] = dpp<0x15B>(pp); } \
        if (t == 3) { pb[0] = dpp<0x15C>(pp); pb[1] = dpp<0x15D>(pp); pb[2] = dpp<0x15E>(pp); pb[3] = dpp<0x15F>(pp); } \
        _Pragma("unroll") for (int h = 0; h < 4; ++h) \
          _Pragma("unroll") for (int k = 0; k < 8; ++k) acc[h * 8 + k] = fmaf(pb[h], vf[k], acc[h * 8 + k]); \
      } } while (0)
    const bool extra = wave <= NPG % SW;
    constexpr int NLOOP = ((PFULL - NB) / NB) * NB;
    for (int i0 = 0; i0 < NLOOP; i0 += NB) {
#pragma unroll
      for (int b = 0; b < NB; ++b) {
        PAGE_BODY(b);
        ISSUE(b, i0 + b + NB);
      }
    }
#pragma unroll
    for (int j = 0; j < PFULL - NLOOP; ++j) {
      PAGE_BODY(j % NB);
      if (NLOOP + j + NB < PFULL || (NLOOP + j + NB == PFULL && extra)) ISSUE(j % NB, NLOOP + j + NB);
    }
    if (extra) PAGE_BODY((PFULL - NLOOP) % NB);
#undef PAGE_BODY
#undef ISSUE
#pragma unroll
    for (int k = 0; k < 32; ++k) acc[k] = xrow16_sum(acc[k]);
    { float t = l; t += dpp<ROR4>(t); t += dpp<XOR8>(t); l = xrow16_sum(t); }
    if (r == 0) {
#pragma unroll
      for (int h = 0; h < 4; ++h) {
        float* rec = mrg + ((wave - 1) * 4 + h) * 132;
        *reinterpret_cast<f32x4*>(rec + 4 + 8 * c) = f32x4{acc[h * 8], acc[h * 8 + 1], acc[h * 8 + 2], acc[h * 8 + 3]};
        *reinterpret_cast<f32x4*>(rec + 8 + 8 * c) = f32x4{acc[h * 8 + 4], acc[h * 8 + 5], acc[h * 8 + 6], acc[h * 8 + 7]};
        if (c == h) { rec[0] = m; rec[1] = l; }
      }
    }
    __syncthreads();
  }
  if (tid < 288) {
    const int h = tid < 256 ? (tid >> 6) : ((tid - 256) >> 3);
    const int d = 2 * (tid & 63);
    float M = -INFINITY;
#pragma unroll
    for (int w = 0; w < SW; ++w) M = fmaxf(M, mrg[(w * 4 + h) * 132]);
    float L = 0.f, o0 = 0.f, o1 = 0.f;
#pragma unroll
    for (int w = 0; w < SW; ++w) { const float* rec = mrg + (w * 4 + h) * 132;
      const float wt = __builtin_amdgcn_exp2f((rec[0] - M) * LOG2E);
      L = fmaf(wt, rec[1], L); o0 = fmaf(wt, rec[4 + d], o0); o1 = fmaf(wt, rec[5 + d], o1); }
    if (tid < 256) GSTORE(WS_PA + ((g * 32 + h * 8 + ((tid & 63) >> 3)) * 32 + s) * 144 + (tid & 7) * 16, f2u(o0), f2u(o1));
    else           GSTORE(WS_PA + ((g * 32 + (tid - 256)) * 32 + s) * 144 + 128, f2u(M), f2u(L));
  }
  float* const inb = reinterpret_cast<float*>(smem + L_IN);
  if (tid < 320) {
    const int t = tid < 288 ? tid : 0;
    u32x4 a;
    for (int spin = 0; spin < SPIN_MAX; ++spin) {
      asm volatile("" ::: "memory");
      a = GLOAD(WS_PA + (g * 32 + s) * (32 * 144) + t * 16);
      if (__all(a[0] == tag && a[2] == tag)) break;
      __builtin_amdgcn_s_sleep(2);
    }
    if (tid < 288) { inb[2 * tid] = u2f(a[1]); inb[2 * tid + 1] = u2f(a[3]); }
  }
  __syncthreads();
  if (tid < 256) {
    const int sp = lane & 31;
    const float ms = inb[sp * 18 + 16], ls = inb[sp * 18 + 17];
    const float M = wave_max(ms);
    const float wt = __builtin_amdgcn_exp2f((ms - M) * LOG2E);
    const float L = wave_sum(lane < 32 ? wt * ls : 0.f);
    float a0 = 0.f, a1 = 0.f;
#pragma unroll
    for (int i = 0; i < 16; ++i) {
      const float oi = bfr(wave_sum(lane < 32 ? wt * inb[sp * 18 + i] : 0.f) / L);
      const unsigned w = *reinterpret_cast<const unsigned*>(smem + L_WO + i * 1024 + tid * 4);
      a0 = fmaf(oi, bf2f(w), a0); a1 = fmaf(oi, bfhi(w), a1);
    }
    GSTORE(WS_AP + wg * 4096 + tid * 16, f2u(a0), f2u(a1));
    u32x4 a;
    for (int spin = 0; spin < SPIN_MAX; ++spin) {
      asm volatile("" ::: "memory");
      a = GLOAD(WS_AP + tid * 4096 + wg * 16);
      if (__all(a[0] == tag && a[2] == tag)) break;
      __builtin_amdgcn_s_sleep(2);
    }
    const float s0 = wave_sum(u2f(a[1])), s1 = wave_sum(u2f(a[3]));
    if (lane == 0) { red[wave * 2] = s0; red[wave * 2 + 1] = s1; }
  }
  __syncthreads();
  if (tid == 0) {
    const float s0 = (red[0] + red[2]) + (red[4] + red[6]), s1 = (red[1] + red[3]) + (red[5] + red[7]);
    const unsigned xx = *reinterpret_cast<const unsigned*>(p.x + 2 * wg);
    const float h0 = bfr(bf2f(xx) + bfr(s0)), h1 = bfr(bfhi(xx) + bfr(s1));
    GSTORE(WS_HG + wg * 16, f2u(h0), f2u(h1));
  }
  if (wg >= NUP) return;
  float* const hs = reinterpret_cast<float*>(smem + L_H);
  if (tid < 256) {
    u32x4 a;
    for (int spin = 0; spin < SPIN_MAX; ++spin) {
      asm volatile("" ::: "memory");
      a = GLOAD(WS_HG + tid * 16);
      if (__all(a[0] == tag && a[2] == tag)) break;
      __builtin_amdgcn_s_sleep(2);
    }
    hs[2 * tid] = u2f(a[1]); hs[2 * tid + 1] = u2f(a[3]);
  }
  __syncthreads();
  {
    float hv[8];
#pragma unroll
    for (int k = 0; k < 8; ++k) hv[k] = hs[lane * 8 + k];
    float mm, dd; ln_stats8(hv, mm, dd);
    const float hn = bfr(bfr(hs[tid] - mm) / dd);
    const u32x4 w = *reinterpret_cast<const u32x4*>(smem + L_W1 + tid * 16);
    float y[8];
#pragma unroll
    for (int k = 0; k < 4; ++k) { y[2 * k] = wave_sum(hn * bf2f(w[k])); y[2 * k + 1] = wave_sum(hn * bfhi(w[k])); }
    if (lane == 0) {
#pragma unroll
      for (int k = 0; k < 8; ++k) red[wave * 8 + k] = y[k]; }
  }
  __syncthreads();
  if (wave == 0) {
    const int k = lane & 7, ju = g * 28 + s;
    float sum = 0.f;
#pragma unroll
    for (int w = 0; w < WAVES; ++w) sum += red[w * 8 + k];
    const float hv = bfr(bfr(sum) + bf2f(p.b1[ju * 8 + k]));
    const float rr = hv > 0.f ? hv : 0.f;
    const unsigned ub = f2bf(rr * rr);
    const unsigned b0 = __shfl(ub, 4 * (lane & 1)), b1 = __shfl(ub, 4 * (lane & 1) + 1), b2 = __shfl(ub, 4 * (lane & 1) + 2), b3 = __shfl(ub, 4 * (lane & 1) + 3);
    if (lane < 2) GSTORE(WS_UG + (ju * 2 + lane) * 16, b0 | (b1 << 16), b2 | (b3 << 16));
  }
  if (wg >= NDOWN) return;
  float* const us = reinterpret_cast<float*>(smem + L_U);
  if (tid < 448) {
    u32x4 a;
    for (int spin = 0; spin < SPIN_MAX; ++spin) {
      asm volatile("" ::: "memory");
      a = GLOAD(WS_UG + tid * 16);
      if (__all(a[0] == tag && a[2] == tag)) break;
      __builtin_amdgcn_s_sleep(2);
    }
    *reinterpret_cast<f32x4*>(us + 4 * tid) = f32x4{bf2f(a[1]), bfhi(a[1]), bf2f(a[3]), bfhi(a[3])};
  }
  __syncthreads();
  {
    float y[8] = {0, 0, 0, 0, 0, 0, 0, 0};
#pragma unroll
    for (int i = 0; i < 4; ++i) {
      const int row = tid + 512 * i;
      if (row < FF) {
        const u32x4 w = *reinterpret_cast<const u32x4*>(smem + L_W2 + row * 16);
        const float uv = us[row];
#pragma unroll
        for (int k = 0; k < 4; ++k) { y[2 * k] = fmaf(uv, bf2f(w[k]), y[2 * k]); y[2 * k + 1] = fmaf(uv, bfhi(w[k]), y[2 * k + 1]); }
      }
    }
#pragma unroll
    for (int k = 0; k < 8; ++k) y[k] = wave_sum(y[k]);
    if (lane == 0) {
#pragma unroll
      for (int k = 0; k < 8; ++k) red[wave * 8 + k] = y[k]; }
  }
  __syncthreads();
  if (tid < 8) {
    const int col = (g * 8 + s) * 8 + tid;
    float sum = 0.f;
#pragma unroll
    for (int w = 0; w < WAVES; ++w) sum += red[w * 8 + tid];
    const float ov = bfr(bfr(sum) + bf2f(p.b2[col]));
    p.out[col] = (bf16_t)f2bf(hs[col] + ov);
  }
  if (wg == 0 && tid == 0) __hip_atomic_store(reinterpret_cast<unsigned*>(p.ws + WS_EPOCH), tag, __ATOMIC_RELAXED, __HIP_MEMORY_SCOPE_AGENT);
}

extern "C" void kernel_launch(void* const* d_in, const int* in_sizes, int n_in,
                              void* d_out, int out_size, void* d_ws, size_t ws_size,
                              hipStream_t stream) {
  Params p;
  p.x  = (const bf16_t*)d_in[0];
  p.kc = (const bf16_t*)d_in[1];
  p.vc = (const bf16_t*)d_in[2];
  p.wq = (const bf16_t*)d_in[3];
  p.wo = (const bf16_t*)d_in[4];
  p.w1 = (const bf16_t*)d_in[6];
  p.b1 = (const bf16_t*)d_in[7];
  p.w2 = (const bf16_t*)d_in[8];
  p.b2 = (const bf16_t*)d_in[9];
  p.out = (bf16_t*)d_out;
  p.ws = (unsigned char*)d_ws;
  fused_decode<<<NWG, NTHR, 0, stream>>>(p);
}
```

```cpp
#include <hip/hip_runtime.h>
#include <hip/hip_bf16.h>
#include <cstdint>

typedef unsigned short bf16_t;
using bf16x2 = __attribute__((ext_vector_type(2))) __bf16;
using u32x4  = __attribute__((ext_vector_type(4))) unsigned;
using u32x2  = __attribute__((ext_vector_type(2))) unsigned;
using f32x4  = __attribute__((ext_vector_type(4))) float;
#define LAS __attribute__((address_space(3)))

constexpr int DM = 512, HQ = 32, HKV = 8, SK = 65536, DH = 128, FF = 1792;
constexpr int NSPLIT = 32, NWG = HKV * NSPLIT, KEYS_PER_WG = SK / NSPLIT, PAGE = 16, WAVES = 8, NTHR = WAVES * 64;
constexpr int PPW = KEYS_PER_WG / PAGE / WAVES;
#define NBUF 3
constexpr int NB = NBUF;
constexpr int UP0 = 0, NUP = FF / 16, RB0 = UP0 + NUP, NRB = DM / 8, DN0 = RB0 + NRB, NDN = DM / 8;
constexpr float SCALE = 0.08838834764831845f;
constexpr float LOG2E = 1.4426950408889634f;
constexpr int SPIN_MAX = 1 << 18;

constexpr unsigned WS_EPOCH = 0;
constexpr unsigned WS_QG = 256;
constexpr unsigned WS_PA = WS_QG + 8 * 32 * 4 * 16;
constexpr unsigned WS_AP = WS_PA + 8 * 32 * 32 * 9 * 16;
constexpr unsigned WS_HG = WS_AP + 256 * 256 * 16;
constexpr unsigned WS_UG = WS_HG + 256 * 16;
constexpr unsigned WS_END = WS_UG + 112 * 4 * 16;

constexpr int L_WO = 0;
constexpr int L_W1 = L_WO + 16384;
constexpr int L_W2 = L_W1;
constexpr int L_MRG = L_W2 + 28672;
constexpr int L_Q = L_MRG + 8 * 4 * 132 * 4;
constexpr int L_RED = L_Q + 1024;
constexpr int L_H = L_RED + 512;
constexpr int L_U = L_H + 2048;
constexpr int L_IN = L_U + 7168;
constexpr int L_END = L_IN + 32 * 18 * 4;

__device__ __forceinline__ float bf2f(unsigned b) { return __builtin_bit_cast(float, b << 16); }
__device__ __forceinline__ float bfhi(unsigned b) { return __builtin_bit_cast(float, b & 0xffff0000u); }
__device__ __forceinline__ float bfr(float x) { return (float)(__bf16)x; }
__device__ __forceinline__ unsigned f2bf(float x) { return (unsigned)__builtin_bit_cast(unsigned short, (__bf16)x); }
__device__ __forceinline__ float u2f(unsigned u) { return __builtin_bit_cast(float, u); }
__device__ __forceinline__ unsigned f2u(float f) { return __builtin_bit_cast(unsigned, f); }
__device__ __forceinline__ float dot2(unsigned k, unsigned q, float acc) {
  return __builtin_amdgcn_fdot2_f32_bf16(__builtin_bit_cast(bf16x2, k), __builtin_bit_cast(bf16x2, q), acc, false);
}
template <int CTRL> __device__ __forceinline__ float dpp(float x) {
  return __builtin_bit_cast(float, __builtin_amdgcn_mov_dpp(__builtin_bit_cast(int, x), CTRL, 0xf, 0xf, true));
}
constexpr int XOR1 = 0xB1, XOR2 = 0x4E, XOR7 = 0x141, XOR8 = 0x128, ROR4 = 0x124;
__device__ __forceinline__ float xrow16_max(float x) {
  auto s = __builtin_amdgcn_permlane16_swap(__float_as_uint(x), __float_as_uint(x), false, false);
  x = fmaxf(__uint_as_float(s[0]), __uint_as_float(s[1]));
  auto t = __builtin_amdgcn_permlane32_swap(__float_as_uint(x), __float_as_uint(x), false, false);
  return fmaxf(__uint_as_float(t[0]), __uint_as_float(t[1]));
}
__device__ __forceinline__ float xrow16_sum(float x) {
  auto s = __builtin_amdgcn_permlane16_swap(__float_as_uint(x), __float_as_uint(x), false, false);
  x = __uint_as_float(s[0]) + __uint_as_float(s[1]);
  auto t = __builtin_amdgcn_permlane32_swap(__float_as_uint(x), __float_as_uint(x), false, false);
  return __uint_as_float(t[0]) + __uint_as_float(t[1]);
}
__device__ __forceinline__ float wave_sum(float x) {
  x += dpp<XOR1>(x); x += dpp<XOR2>(x); x += dpp<XOR7>(x); x += dpp<XOR8>(x);
  return xrow16_sum(x);
}
__device__ __forceinline__ float wave_max(float x) {
  x = fmaxf(x, dpp<XOR1>(x)); x = fmaxf(x, dpp<XOR2>(x)); x = fmaxf(x, dpp<XOR7>(x)); x = fmaxf(x, dpp<XOR8>(x));
  return xrow16_max(x);
}
__device__ __forceinline__ void ln_stats8(const float (&v)[8], float& m_bf, float& d_bf) {
  float s = 0.f;
#pragma unroll
  for (int k = 0; k < 8; ++k) s += v[k];
  const float mean = wave_sum(s) * (1.f / DM);
  float q = 0.f;
#pragma unroll
  for (int k = 0; k < 8; ++k) { const float d = v[k] - mean; q = fmaf(d, d, q); }
  const float var = bfr(wave_sum(q) * (1.f / DM));
  m_bf = bfr(mean);
  d_bf = bfr(sqrtf(bfr(var + 1e-5f)));
}
__device__ __forceinline__ void glds16(const void* gsrc, unsigned lds_dst) {
  unsigned keep;
  asm volatile("s_mov_b32 %0, m0\n\ts_mov_b32 m0, %2\n\ts_nop 0\n\tglobal_load_lds_dwordx4 %1, off\n\ts_mov_b32 m0, %0"
               : "=&s"(keep) : "v"(gsrc), "s"(lds_dst) : "memory");
}

struct Params { const bf16_t *x, *kc, *vc, *wq, *wo, *w1, *b1, *w2, *b2; bf16_t* out; unsigned char* ws; };

#define STAMP(i_) do { } while (0)
#define GSTORE(off_, d0_, d1_) __builtin_amdgcn_raw_buffer_store_b128(u32x4{tag, (d0_), tag, (d1_)}, rsrc, (off_), 0, 16)
#define GLOAD(off_) __builtin_amdgcn_raw_buffer_load_b128(rsrc, (off_), 0, 16)

__global__ void __launch_bounds__(NTHR, 2) fused_decode(Params p) {
  __shared__ __attribute__((aligned(16))) unsigned char smem[L_END];
  const int wg = blockIdx.x, g = wg & 7, s = wg >> 3;
  const int tid = threadIdx.x, lane = tid & 63, wave = __builtin_amdgcn_readfirstlane(tid >> 6), r = lane >> 4, c = lane & 15;
  const auto rsrc = __builtin_amdgcn_make_buffer_rsrc(p.ws, 0, WS_END, 0x00020000);
  float* const red = reinterpret_cast<float*>(smem + L_RED);

  if (wave == 0) STAMP(0);
  unsigned ev = __hip_atomic_load(reinterpret_cast<unsigned*>(p.ws + WS_EPOCH), __ATOMIC_RELAXED, __HIP_MEMORY_SCOPE_AGENT);
  u32x4 xv = *reinterpret_cast<const u32x4*>(p.x + 8 * lane);
  unsigned xt = p.x[tid];
  const bf16_t* wqp = p.wq + (size_t)tid * (HQ * DH) + g * 512 + s * 16;
  u32x4 wq0 = *reinterpret_cast<const u32x4*>(wqp), wq1 = *reinterpret_cast<const u32x4*>(wqp + 8);

  __builtin_amdgcn_s_barrier();
  constexpr int SW = WAVES - 1;
  constexpr int NPG = KEYS_PER_WG / PAGE;
  constexpr int PFULL = NPG / SW;
  float* const mrg = reinterpret_cast<float*>(smem + L_MRG);
#define TR_STEP(x, HALF, CTRL, BIT) _Pragma("unroll") for (int hh = 0; hh < (HALF); ++hh) { \
      const float lo_ = x[hh], hi_ = x[hh + (HALF)]; const float t1_ = lo_ + dpp<CTRL>(lo_), t2_ = hi_ + dpp<CTRL>(hi_); \
      x[hh] = (c & (BIT)) ? t2_ : t1_; }
  auto qpartial = [&]() {
    float xf[8];
#pragma unroll
    for (int k = 0; k < 4; ++k) { xf[2 * k] = bf2f(xv[k] & 0xffffu); xf[2 * k + 1] = bfhi(xv[k]); }
    float m, d; ln_stats8(xf, m, d);
    const float xn = bfr(bfr(bf2f(xt) - m) / d);
    float y[16];
#pragma unroll
    for (int k = 0; k < 4; ++k) { y[2 * k] = xn * bf2f(wq0[k] & 0xffffu); y[2 * k + 1] = xn * bfhi(wq0[k]);
                                  y[8 + 2 * k] = xn * bf2f(wq1[k] & 0xffffu); y[9 + 2 * k] = xn * bfhi(wq1[k]); }
    TR_STEP(y, 8, XOR8, 8) TR_STEP(y, 4, XOR7, 4) TR_STEP(y, 2, XOR2, 2) TR_STEP(y, 1, XOR1, 1)
    const float t = xrow16_sum(y[0]);
    if (r == 0) red[wave * 16 + c] = t;
  };
  unsigned tag;
  if (wave == 0) {
    asm volatile("; service wave" : "+v"(ev), "+v"(xv), "+v"(xt), "+v"(wq0), "+v"(wq1) :: "memory");
    qpartial();
    tag = __builtin_amdgcn_readfirstlane(ev) + 1u;
    __syncthreads();
    STAMP(1);
    float qs = 0.f;
#pragma unroll
    for (int w = 0; w < WAVES; ++w) qs += red[w * 16 + c];
    const unsigned qb = f2bf(qs);
    const unsigned b0 = __shfl(qb, 4 * (lane & 3)), b1 = __shfl(qb, 4 * (lane & 3) + 1), b2 = __shfl(qb, 4 * (lane & 3) + 2), b3 = __shfl(qb, 4 * (lane & 3) + 3);
    if (lane < 4) GSTORE(WS_QG + ((g * 32 + s) * 4 + lane) * 16, b0 | (b1 << 16), b2 | (b3 << 16));
    u32x4 a0, a1;
    for (int spin = 0; spin < SPIN_MAX; ++spin) {
      asm volatile("" ::: "memory");
      a0 = GLOAD(WS_QG + g * 2048 + lane * 16); a1 = GLOAD(WS_QG + g * 2048 + 1024 + lane * 16);
      const bool ok = a0[0] == tag && a0[2] == tag && a1[0] == tag && a1[2] == tag;
      if (__all(ok)) break;
      __builtin_amdgcn_s_sleep(1);
    }
    STAMP(2);
    *reinterpret_cast<u32x2*>(smem + L_Q + lane * 8) = u32x2{a0[1], a0[3]};
    *reinterpret_cast<u32x2*>(smem + L_Q + 512 + lane * 8) = u32x2{a1[1], a1[3]};
    __syncthreads();
    const unsigned lbase = (unsigned)(uintptr_t)(LAS unsigned char*)smem;
    const bf16_t* wop = p.wo + (size_t)(g * 512 + s * 16) * DM + 8 * lane;
#pragma unroll
    for (int i = 0; i < 16; ++i) glds16(wop + i * DM, lbase + L_WO + i * 1024);
    asm volatile("s_waitcnt vmcnt(0)" ::: "memory");
    __syncthreads();
  } else {
    const size_t base = ((size_t)g * SK + (size_t)s * KEYS_PER_WG + (size_t)(wave - 1) * PAGE + r) * DH + 8 * c;
    const bf16_t* kp = p.kc + base;
    const bf16_t* vp = p.vc + base;
    u32x4 Kb[NB][4], Vb[NB][4];
#define ISSUE(b_, i_) do { _Pragma("unroll") for (int t_ = 0; t_ < 4; ++t_) { \
    Kb[b_][t_] = __builtin_nontemporal_load(reinterpret_cast<const u32x4*>(kp + (size_t)(i_) * (SW * PAGE * DH) + t_ * 4 * DH)); \
    Vb[b_][t_] = __builtin_nontemporal_load(reinterpret_cast<const u32x4*>(vp + (size_t)(i_) * (SW * PAGE * DH) + t_ * 4 * DH)); } } while (0)
    ISSUE(0, 0);
    __builtin_amdgcn_sched_barrier(0);
    asm volatile("; streaming waves" : "+v"(ev), "+v"(xv), "+v"(xt), "+v"(wq0), "+v"(wq1) :: "memory");
    qpartial();
    tag = __builtin_amdgcn_readfirstlane(ev) + 1u;
    __syncthreads();
    ISSUE(1, 1);
#pragma unroll
    for (int b = 2; b < NB; ++b) { asm volatile("s_waitcnt vmcnt(8)" ::: "memory"); ISSUE(b, b); }
    __syncthreads();
    u32x4 qpk[4];
#pragma unroll
    for (int h = 0; h < 4; ++h) qpk[h] = *reinterpret_cast<const u32x4*>(smem + L_Q + h * 256 + 16 * c);
    float m = -INFINITY, l = 0.f, acc[32];
#pragma unroll
    for (int k = 0; k < 32; ++k) acc[k] = 0.f;
#define PAGE_BODY(b) do { \
      float x[16]; \
      _Pragma("unroll") for (int t = 0; t < 4; ++t) \
        _Pragma("unroll") for (int h = 0; h < 4; ++h) { float d = 0.f; \
          _Pragma("unroll") for (int k = 0; k < 4; ++k) d = dot2(Kb[b][t][k], qpk[h][k], d); \
          x[t * 4 + h] = d; } \
      TR_STEP(x, 8, XOR8, 8) TR_STEP(x, 4, XOR7, 4) TR_STEP(x, 2, XOR2, 2) TR_STEP(x, 1, XOR1, 1) \
      const float sc = bfr(bfr(x[0]) * SCALE); \
      float pm = sc; pm = fmaxf(pm, dpp<ROR4>(pm)); pm = fmaxf(pm, dpp<XOR8>(pm)); pm = xrow16_max(pm); \
      if (__any(pm > m)) { \
        const float mn = fmaxf(m, pm); \
        const float alpha = __builtin_amdgcn_exp2f((m - mn) * LOG2E); \
        l *= alpha; m = mn; \
        const float a0 = dpp<0x00>(alpha), a1 = dpp<0x55>(alpha), a2 = dpp<0xAA>(alpha), a3 = dpp<0xFF>(alpha); \
        _Pragma("unroll") for (int k = 0; k < 8; ++k) { acc[k] *= a0; acc[8 + k] *= a1; acc[16 + k] *= a2; acc[24 + k] *= a3; } \
      } \
      const float pp = __builtin_amdgcn_exp2f((sc - m) * LOG2E); \
      l += pp; \
      _Pragma("unroll") for (int t = 0; t < 4; ++t) { \
        float vf[8]; \
        _Pragma("unroll") for (int k = 0; k < 4; ++k) { vf[2 * k] = bf2f(Vb[b][t][k]); vf[2 * k + 1] = bfhi(Vb[b][t][k]); } \
        float pb[4]; \
        if (t == 0) { pb[0] = dpp<0x150>(pp); pb[1] = dpp<0x151>(pp); pb[2] = dpp<0x152>(pp); pb[3] = dpp<0x153>(pp); } \
        if (t == 1) { pb[0] = dpp<0x154>(pp); pb[1] = dpp<0x155>(pp); pb[2] = dpp<0x156>(pp); pb[3] = dpp<0x157>(pp); } \
        if (t == 2) { pb[0] = dpp<0x158>(pp); pb[1] = dpp<0x159>(pp); pb[2] = dpp<0x15A>(pp); pb[3] = dpp<0x15B>(pp); } \
        if (t == 3) { pb[0] = dpp<0x15C>(pp); pb[1] = dpp<0x15D>(pp); pb[2] = dpp<0x15E>(pp); pb[3] = dpp<0x15F>(pp); } \
        _Pragma("unroll") for (int h = 0; h < 4; ++h) \
          _Pragma("unroll") for (int k = 0; k < 8; ++k) acc[h * 8 + k] = fmaf(pb[h], vf[k], acc[h * 8 + k]); \
      } } while (0)
    const bool extra = wave <= NPG % SW;
    constexpr int NLOOP = ((PFULL - NB) / NB) * NB;
    for (int i0 = 0; i0 < NLOOP; i0 += NB) {
#pragma unroll
      for (int b = 0; b < NB; ++b) {
        PAGE_BODY(b);
        ISSUE(b, i0 + b + NB);
      }
    }
#pragma unroll
    for (int j = 0; j < PFULL - NLOOP; ++j) {
      PAGE_BODY(j % NB);
      if (NLOOP + j + NB < PFULL || (NLOOP + j + NB == PFULL && extra)) ISSUE(j % NB, NLOOP + j + NB);
    }
    if (extra) PAGE_BODY((PFULL - NLOOP) % NB);
#undef PAGE_BODY
#undef ISSUE
    if (wave == 1) STAMP(3);
    if (wave == 7) STAMP(10);
#pragma unroll
    for (int k = 0; k < 32; ++k) acc[k] = xrow16_sum(acc[k]);
    { float t = l; t += dpp<ROR4>(t); t += dpp<XOR8>(t); l = xrow16_sum(t); }
    if (r == 0) {
#pragma unroll
      for (int h = 0; h < 4; ++h) {
        float* rec = mrg + ((wave - 1) * 4 + h) * 132;
        *reinterpret_cast<f32x4*>(rec + 4 + 8 * c) = f32x4{acc[h * 8], acc[h * 8 + 1], acc[h * 8 + 2], acc[h * 8 + 3]};
        *reinterpret_cast<f32x4*>(rec + 8 + 8 * c) = f32x4{acc[h * 8 + 4], acc[h * 8 + 5], acc[h * 8 + 6], acc[h * 8 + 7]};
        if (c == h) { rec[0] = m; rec[1] = l; }
      }
    }
    __syncthreads();
  }
  if (wave == 0) STAMP(4);
  if (tid < 288) {
    const int h = tid < 256 ? (tid >> 6) : ((tid - 256) >> 3);
    const int d = 2 * (tid & 63);
    float M = -INFINITY;
#pragma unroll
    for (int w = 0; w < SW; ++w) M = fmaxf(M, mrg[(w * 4 + h) * 132]);
    float L = 0.f, o0 = 0.f, o1 = 0.f;
#pragma unroll
    for (int w = 0; w < SW; ++w) { const float* rec = mrg + (w * 4 + h) * 132;
      const float wt = __builtin_amdgcn_exp2f((rec[0] - M) * LOG2E);
      L = fmaf(wt, rec[1], L); o0 = fmaf(wt, rec[4 + d], o0); o1 = fmaf(wt, rec[5 + d], o1); }
    if (tid < 256) GSTORE(WS_PA + ((g * 32 + h * 8 + ((tid & 63) >> 3)) * 32 + s) * 144 + (tid & 7) * 16, f2u(o0), f2u(o1));
    else           GSTORE(WS_PA + ((g * 32 + (tid - 256)) * 32 + s) * 144 + 128, f2u(M), f2u(L));
  }
  float* const inb = reinterpret_cast<float*>(smem + L_IN);
  if (tid < 320) {
    const int t = tid < 288 ? tid : 0;
    u32x4 a;
    for (int spin = 0; spin < SPIN_MAX; ++spin) {
      asm volatile("" ::: "memory");
      a = GLOAD(WS_PA + (g * 32 + s) * (32 * 144) + t * 16);
      if (__all(a[0] == tag && a[2] == tag)) break;
      __builtin_amdgcn_s_sleep(2);
    }
    if (tid < 288) { inb[2 * tid] = u2f(a[1]); inb[2 * tid + 1] = u2f(a[3]); }
  }
  __syncthreads();
  if (wave == 0) STAMP(5);
  if (tid < 256) {
    const int sp = lane & 31;
    const float ms = inb[sp * 18 + 16], ls = inb[sp * 18 + 17];
    const float M = wave_max(ms);
    const float wt = __builtin_amdgcn_exp2f((ms - M) * LOG2E);
    const float L = wave_sum(lane < 32 ? wt * ls : 0.f);
    float a0 = 0.f, a1 = 0.f;
#pragma unroll
    for (int i = 0; i < 16; ++i) {
      const float oi = bfr(wave_sum(lane < 32 ? wt * inb[sp * 18 + i] : 0.f) / L);
      const unsigned w = *reinterpret_cast<const unsigned*>(smem + L_WO + i * 1024 + tid * 4);
      a0 = fmaf(oi, bf2f(w), a0); a1 = fmaf(oi, bfhi(w), a1);
    }
    GSTORE(WS_AP + ((tid >> 2) * 256 + wg) * 64 + (tid & 3) * 16, f2u(a0), f2u(a1));
  }
  const unsigned lbase = (unsigned)(uintptr_t)(LAS unsigned char*)smem;
  if (wg < RB0) {
    const int ju = (wg & 7) * 14 + (wg >> 3);
    {
      const bf16_t* w1p = p.w1 + (size_t)tid * FF + ju * 16;
      glds16(w1p, lbase + L_W1 + wave * 1024);
      glds16(w1p + 8, lbase + L_W1 + 8192 + wave * 1024);
    }
    float* const hs = reinterpret_cast<float*>(smem + L_H);
    if (tid < 256) {
      u32x4 a;
      for (int spin = 0; spin < SPIN_MAX; ++spin) {
        asm volatile("" ::: "memory");
        a = GLOAD(WS_HG + tid * 16);
        if (__all(a[0] == tag && a[2] == tag)) break;
        __builtin_amdgcn_s_sleep(2);
      }
      hs[2 * tid] = u2f(a[1]); hs[2 * tid + 1] = u2f(a[3]);
    }
    asm volatile("s_waitcnt vmcnt(0)" ::: "memory");
    __syncthreads();
    if (wave == 0) STAMP(7);
    {
      float hv[8];
#pragma unroll
      for (int k = 0; k < 8; ++k) hv[k] = hs[lane * 8 + k];
      float mm, dd; ln_stats8(hv, mm, dd);
      const float hn = bfr(bfr(hs[tid] - mm) / dd);
      const u32x4 w0 = *reinterpret_cast<const u32x4*>(smem + L_W1 + tid * 16), w1 = *reinterpret_cast<const u32x4*>(smem + L_W1 + 8192 + tid * 16);
      float y[16];
#pragma unroll
      for (int k = 0; k < 4; ++k) { y[2 * k] = hn * bf2f(w0[k]); y[2 * k + 1] = hn * bfhi(w0[k]); y[8 + 2 * k] = hn * bf2f(w1[k]); y[9 + 2 * k] = hn * bfhi(w1[k]); }
      TR_STEP(y, 8, XOR8, 8) TR_STEP(y, 4, XOR7, 4) TR_STEP(y, 2, XOR2, 2) TR_STEP(y, 1, XOR1, 1)
      const float t = xrow16_sum(y[0]);
      if (r == 0) red[wave * 16 + c] = t;
    }
    __syncthreads();
    if (wave == 0) {
      float sum = 0.f;
#pragma unroll
      for (int w = 0; w < WAVES; ++w) sum += red[w * 16 + c];
      const float hv = bfr(bfr(sum) + bf2f(p.b1[ju * 16 + c]));
      const float rr = hv > 0.f ? hv : 0.f;
      const unsigned ub = f2bf(rr * rr);
      const unsigned b0 = __shfl(ub, 4 * (lane & 3)), b1 = __shfl(ub, 4 * (lane & 3) + 1), b2 = __shfl(ub, 4 * (lane & 3) + 2), b3 = __shfl(ub, 4 * (lane & 3) + 3);
      if (lane < 4) GSTORE(WS_UG + (ju * 4 + lane) * 16, b0 | (b1 << 16), b2 | (b3 << 16));
    }
  } else if (wg < DN0) {
    const int jb = wg - RB0;
    u32x4 a, b;
    for (int spin = 0; spin < SPIN_MAX; ++spin) {
      asm volatile("" ::: "memory");
      a = GLOAD(WS_AP + jb * 16384 + tid * 16); b = GLOAD(WS_AP + jb * 16384 + 8192 + tid * 16);
      if (__all(a[0] == tag && a[2] == tag && b[0] == tag && b[2] == tag)) break;
      __builtin_amdgcn_s_sleep(2);
    }
    float s0 = u2f(a[1]) + u2f(b[1]), s1 = u2f(a[3]) + u2f(b[3]);
    s0 += dpp<ROR4>(s0); s0 += dpp<XOR8>(s0); s0 = xrow16_sum(s0);
    s1 += dpp<ROR4>(s1); s1 += dpp<XOR8>(s1); s1 = xrow16_sum(s1);
    if (lane < 4) { red[wave * 8 + 2 * lane] = s0; red[wave * 8 + 2 * lane + 1] = s1; }
    __syncthreads();
    if (wave == 0) STAMP(6);
    if (tid < 4) {
      float t0 = 0.f, t1 = 0.f;
#pragma unroll
      for (int w = 0; w < WAVES; ++w) { t0 += red[w * 8 + 2 * tid]; t1 += red[w * 8 + 2 * tid + 1]; }
      const unsigned xx = *reinterpret_cast<const unsigned*>(p.x + jb * 8 + 2 * tid);
      const float h0 = bfr(bf2f(xx) + bfr(t0)), h1 = bfr(bfhi(xx) + bfr(t1));
      GSTORE(WS_HG + (jb * 4 + tid) * 16, f2u(h0), f2u(h1));
    }
  } else if (wg < DN0 + NDN) {
    const int jd = (wg & 7) * 8 + ((wg - DN0) >> 3);
    {
      const bf16_t* w2p = p.w2 + (size_t)tid * DM + jd * 8;
      glds16(w2p, lbase + L_W2 + wave * 1024);
      glds16(w2p + (size_t)512 * DM, lbase + L_W2 + 8192 + wave * 1024);
      glds16(w2p + (size_t)1024 * DM, lbase + L_W2 + 16384 + wave * 1024);
      if (wave < 4) glds16(w2p + (size_t)1536 * DM, lbase + L_W2 + 24576 + wave * 1024);
    }
    float* const us = reinterpret_cast<float*>(smem + L_U);
    float* const hs = reinterpret_cast<float*>(smem + L_H);
    {
      const unsigned off = tid < 448 ? WS_UG + tid * 16 : WS_HG + (jd * 4 + (tid & 3)) * 16;
      u32x4 a;
      for (int spin = 0; spin < SPIN_MAX; ++spin) {
        asm volatile("" ::: "memory");
        a = GLOAD(off);
        if (__all(a[0] == tag && a[2] == tag)) break;
        __builtin_amdgcn_s_sleep(2);
      }
      if (tid < 448) *reinterpret_cast<f32x4*>(us + 4 * tid) = f32x4{bf2f(a[1]), bfhi(a[1]), bf2f(a[3]), bfhi(a[3])};
      else if (tid < 452) { hs[2 * (tid & 3)] = u2f(a[1]); hs[2 * (tid & 3) + 1] = u2f(a[3]); }
    }
    asm volatile("s_waitcnt vmcnt(0)" ::: "memory");
    __syncthreads();
    if (wave == 0) STAMP(8);
    {
      float y[8] = {0, 0, 0, 0, 0, 0, 0, 0};
#pragma unroll
      for (int i = 0; i < 4; ++i) {
        const int row = tid + 512 * i;
        if (row < FF) {
          const u32x4 w = *reinterpret_cast<const u32x4*>(smem + L_W2 + row * 16);
          const float uv = us[row];
#pragma unroll
          for (int k = 0; k < 4; ++k) { y[2 * k] = fmaf(uv, bf2f(w[k]), y[2 * k]); y[2 * k + 1] = fmaf(uv, bfhi(w[k]), y[2 * k + 1]); }
        }
      }
#pragma unroll
      for (int k = 0; k < 8; ++k) y[k] = wave_sum(y[k]);
      if (lane == 0) {
#pragma unroll
        for (int k = 0; k < 8; ++k) red[wave * 8 + k] = y[k]; }
    }
    __syncthreads();
    if (tid < 8) {
      const int col = jd * 8 + tid;
      float sum = 0.f;
#pragma unroll
      for (int w = 0; w < WAVES; ++w) sum += red[w * 8 + tid];
      const float ov = bfr(bfr(sum) + bf2f(p.b2[col]));
      p.out[col] = (bf16_t)f2bf(hs[tid] + ov);
    }
    if (wave == 0) STAMP(9);
    if (wg == DN0 && tid == 0) __hip_atomic_store(reinterpret_cast<unsigned*>(p.ws + WS_EPOCH), tag, __ATOMIC_RELAXED, __HIP_MEMORY_SCOPE_AGENT);
  }
}

extern "C" void kernel_launch(void* const* d_in, const int* in_sizes, int n_in,
                              void* d_out, int out_size, void* d_ws, size_t ws_size,
                              hipStream_t stream) {
  Params p;
  p.x  = (const bf16_t*)d_in[0];
  p.kc = (const bf16_t*)d_in[1];
  p.vc = (const bf16_t*)d_in[2];
  p.wq = (const bf16_t*)d_in[3];
  p.wo = (const bf16_t*)d_in[4];
  p.w1 = (const bf16_t*)d_in[6];
  p.b1 = (const bf16_t*)d_in[7];
  p.w2 = (const bf16_t*)d_in[8];
  p.b2 = (const bf16_t*)d_in[9];
  p.out = (bf16_t*)d_out;
  p.ws = (unsigned char*)d_ws;
  fused_decode<<<NWG, NTHR, 0, stream>>>(p);
}
```

```cpp
#include <hip/hip_runtime.h>
#include <hip/hip_bf16.h>
#include <cstdint>

typedef unsigned short bf16_t;
using bf16x2 = __attribute__((ext_vector_type(2))) __bf16;
using u32x4  = __attribute__((ext_vector_type(4))) unsigned;
using u32x2  = __attribute__((ext_vector_type(2))) unsigned;
using f32x4  = __attribute__((ext_vector_type(4))) float;
#define LAS __attribute__((address_space(3)))

constexpr int DM = 512, HQ = 32, HKV = 8, SK = 65536, DH = 128, FF = 1792;
constexpr int NSPLIT = 32, NWG = HKV * NSPLIT, KEYS_PER_WG = SK / NSPLIT, PAGE = 16, WAVES = 8, NTHR = WAVES * 64;
constexpr int PPW = KEYS_PER_WG / PAGE / WAVES;
#define NBUF 3
constexpr int NB = NBUF;
constexpr int UP0 = 0, NUP = FF / 64, RB0 = UP0 + NUP, NRB = DM / 8, DN0 = RB0 + NRB, NDN = DM / 32;
constexpr float SCALE = 0.08838834764831845f;
constexpr float LOG2E = 1.4426950408889634f;
constexpr int SPIN_MAX = 1 << 18;

constexpr unsigned WS_EPOCH = 0;
constexpr unsigned WS_QG = 256;
constexpr unsigned WS_PA = WS_QG + 8 * 32 * 4 * 16;
constexpr unsigned WS_AP = WS_PA + 8 * 32 * 32 * 9 * 16;
constexpr unsigned WS_HG = WS_AP + 256 * 256 * 16;
constexpr unsigned WS_UG = WS_HG + 256 * 16;
constexpr unsigned WS_END = WS_UG + 112 * 4 * 16;

constexpr int L_WO = 0;
constexpr int L_W1 = L_WO + 16384;
constexpr int L_W2 = L_W1;
constexpr int L_MRG = L_W2 + 114688;
constexpr int L_Q = L_MRG + 8 * 4 * 132 * 4;
constexpr int L_RED = L_Q + 1024;
constexpr int L_H = L_RED + 2048;
constexpr int L_U = L_H + 2048;
constexpr int L_IN = L_U + 7168;
constexpr int L_END = L_IN + 32 * 18 * 4;

__device__ __forceinline__ float bf2f(unsigned b) { return __builtin_bit_cast(float, b << 16); }
__device__ __forceinline__ float bfhi(unsigned b) { return __builtin_bit_cast(float, b & 0xffff0000u); }
__device__ __forceinline__ float bfr(float x) { return (float)(__bf16)x; }
__device__ __forceinline__ unsigned f2bf(float x) { return (unsigned)__builtin_bit_cast(unsigned short, (__bf16)x); }
__device__ __forceinline__ float u2f(unsigned u) { return __builtin_bit_cast(float, u); }
__device__ __forceinline__ unsigned f2u(float f) { return __builtin_bit_cast(unsigned, f); }
__device__ __forceinline__ float dot2(unsigned k, unsigned q, float acc) {
  return __builtin_amdgcn_fdot2_f32_bf16(__builtin_bit_cast(bf16x2, k), __builtin_bit_cast(bf16x2, q), acc, false);
}
template <int CTRL> __device__ __forceinline__ float dpp(float x) {
  return __builtin_bit_cast(float, __builtin_amdgcn_mov_dpp(__builtin_bit_cast(int, x), CTRL, 0xf, 0xf, true));
}
constexpr int XOR1 = 0xB1, XOR2 = 0x4E, XOR7 = 0x141, XOR8 = 0x128, ROR4 = 0x124;
__device__ __forceinline__ float xrow16_max(float x) {
  auto s = __builtin_amdgcn_permlane16_swap(__float_as_uint(x), __float_as_uint(x), false, false);
  x = fmaxf(__uint_as_float(s[0]), __uint_as_float(s[1]));
  auto t = __builtin_amdgcn_permlane32_swap(__float_as_uint(x), __float_as_uint(x), false, false);
  return fmaxf(__uint_as_float(t[0]), __uint_as_float(t[1]));
}
__device__ __forceinline__ float xrow16_sum(float x) {
  auto s = __builtin_amdgcn_permlane16_swap(__float_as_uint(x), __float_as_uint(x), false, false);
  x = __uint_as_float(s[0]) + __uint_as_float(s[1]);
  auto t = __builtin_amdgcn_permlane32_swap(__float_as_uint(x), __float_as_uint(x), false, false);
  return __uint_as_float(t[0]) + __uint_as_float(t[1]);
}
__device__ __forceinline__ float wave_sum(float x) {
  x += dpp<XOR1>(x); x += dpp<XOR2>(x); x += dpp<XOR7>(x); x += dpp<XOR8>(x);
  return xrow16_sum(x);
}
__device__ __forceinline__ float wave_max(float x) {
  x = fmaxf(x, dpp<XOR1>(x)); x = fmaxf(x, dpp<XOR2>(x)); x = fmaxf(x, dpp<XOR7>(x)); x = fmaxf(x, dpp<XOR8>(x));
  return xrow16_max(x);
}
__device__ __forceinline__ void ln_stats8(const float (&v)[8], float& m_bf, float& d_bf) {
  float s = 0.f;
#pragma unroll
  for (int k = 0; k < 8; ++k) s += v[k];
  const float mean = wave_sum(s) * (1.f / DM);
  float q = 0.f;
#pragma unroll
  for (int k = 0; k < 8; ++k) { const float d = v[k] - mean; q = fmaf(d, d, q); }
  const float var = bfr(wave_sum(q) * (1.f / DM));
  m_bf = bfr(mean);
  d_bf = bfr(sqrtf(bfr(var + 1e-5f)));
}
__device__ __forceinline__ void glds16(const void* gsrc, unsigned lds_dst) {
  unsigned keep;
  asm volatile("s_mov_b32 %0, m0\n\ts_mov_b32 m0, %2\n\ts_nop 0\n\tglobal_load_lds_dwordx4 %1, off\n\ts_mov_b32 m0, %0"
               : "=&s"(keep) : "v"(gsrc), "s"(lds_dst) : "memory");
}

struct Params { const bf16_t *x, *kc, *vc, *wq, *wo, *w1, *b1, *w2, *b2; bf16_t* out; unsigned char* ws; };

#define STAMP(i_) do { } while (0)
#define GSTORE(off_, d0_, d1_) __builtin_amdgcn_raw_buffer_store_b128(u32x4{tag, (d0_), tag, (d1_)}, rsrc, (off_), 0, 16)
#define GLOAD(off_) __builtin_amdgcn_raw_buffer_load_b128(rsrc, (off_), 0, 16)

__global__ void __launch_bounds__(NTHR, 2) fused_decode(Params p) {
  __shared__ __attribute__((aligned(16))) unsigned char smem[L_END];
  const int wg = blockIdx.x, g = (wg >> 3) & 7, s = (wg & 7) * 4 + (wg >> 6);
  const int tid = threadIdx.x, lane = tid & 63, wave = __builtin_amdgcn_readfirstlane(tid >> 6), r = lane >> 4, c = lane & 15;
  const auto rsrc = __builtin_amdgcn_make_buffer_rsrc(p.ws, 0, WS_END, 0x00020000);
  float* const red = reinterpret_cast<float*>(smem + L_RED);

  if (wave == 0) STAMP(0);
  unsigned ev = __hip_atomic_load(reinterpret_cast<unsigned*>(p.ws + WS_EPOCH), __ATOMIC_RELAXED, __HIP_MEMORY_SCOPE_AGENT);
  u32x4 xv = *reinterpret_cast<const u32x4*>(p.x + 8 * lane);
  unsigned xt = p.x[tid];
  const bf16_t* wqp = p.wq + (size_t)tid * (HQ * DH) + g * 512 + s * 16;
  u32x4 wq0 = *reinterpret_cast<const u32x4*>(wqp), wq1 = *reinterpret_cast<const u32x4*>(wqp + 8);

  __builtin_amdgcn_s_barrier();
  constexpr int SW = WAVES - 1;
  constexpr int NPG = KEYS_PER_WG / PAGE;
  constexpr int PFULL = NPG / SW;
  float* const mrg = reinterpret_cast<float*>(smem + L_MRG);
#define TR_STEP(x, HALF, CTRL, BIT) _Pragma("unroll") for (int hh = 0; hh < (HALF); ++hh) { \
      const float lo_ = x[hh], hi_ = x[hh + (HALF)]; const float t1_ = lo_ + dpp<CTRL>(lo_), t2_ = hi_ + dpp<CTRL>(hi_); \
      x[hh] = (c & (BIT)) ? t2_ : t1_; }
  auto qpartial = [&]() {
    float xf[8];
#pragma unroll
    for (int k = 0; k < 4; ++k) { xf[2 * k] = bf2f(xv[k] & 0xffffu); xf[2 * k + 1] = bfhi(xv[k]); }
    float m, d; ln_stats8(xf, m, d);
    const float xn = bfr(bfr(bf2f(xt) - m) / d);
    float y[16];
#pragma unroll
    for (int k = 0; k < 4; ++k) { y[2 * k] = xn * bf2f(wq0[k] & 0xffffu); y[2 * k + 1] = xn * bfhi(wq0[k]);
                                  y[8 + 2 * k] = xn * bf2f(wq1[k] & 0xffffu); y[9 + 2 * k] = xn * bfhi(wq1[k]); }
    TR_STEP(y, 8, XOR8, 8) TR_STEP(y, 4, XOR7, 4) TR_STEP(y, 2, XOR2, 2) TR_STEP(y, 1, XOR1, 1)
    const float t = xrow16_sum(y[0]);
    if (r == 0) red[wave * 16 + c] = t;
  };
  unsigned tag;
  if (wave == 0) {
    asm volatile("; service wave" : "+v"(ev), "+v"(xv), "+v"(xt), "+v"(wq0), "+v"(wq1) :: "memory");
    qpartial();
    tag = __builtin_amdgcn_readfirstlane(ev) + 1u;
    __syncthreads();
    STAMP(1);
    float qs = 0.f;
#pragma unroll
    for (int w = 0; w < WAVES; ++w) qs += red[w * 16 + c];
    const unsigned qb = f2bf(qs);
    const unsigned b0 = __shfl(qb, 4 * (lane & 3)), b1 = __shfl(qb, 4 * (lane & 3) + 1), b2 = __shfl(qb, 4 * (lane & 3) + 2), b3 = __shfl(qb, 4 * (lane & 3) + 3);
    if (lane < 4) GSTORE(WS_QG + ((g * 32 + s) * 4 + lane) * 16, b0 | (b1 << 16), b2 | (b3 << 16));
    u32x4 a0, a1;
#pragma unroll 1
    for (int spin = 0; spin < SPIN_MAX; ++spin) {
      asm volatile("" ::: "memory");
      a0 = GLOAD(WS_QG + g * 2048 + lane * 16); a1 = GLOAD(WS_QG + g * 2048 + 1024 + lane * 16);
      const bool ok = a0[0] == tag && a0[2] == tag && a1[0] == tag && a1[2] == tag;
      if (__all(ok)) break;
      __builtin_amdgcn_s_sleep(1);
    }
    STAMP(2);
    *reinterpret_cast<u32x2*>(smem + L_Q + lane * 8) = u32x2{a0[1], a0[3]};
    *reinterpret_cast<u32x2*>(smem + L_Q + 512 + lane * 8) = u32x2{a1[1], a1[3]};
    __syncthreads();
    const unsigned lbase = (unsigned)(uintptr_t)(LAS unsigned char*)smem;
    const bf16_t* wop = p.wo + (size_t)(g * 512 + s * 16) * DM + 8 * lane;
#pragma unroll
    for (int i = 0; i < 16; ++i) glds16(wop + i * DM, lbase + L_WO + i * 1024);
    asm volatile("s_waitcnt vmcnt(0)" ::: "memory");
    __syncthreads();
  } else {
    const size_t base = ((size_t)g * SK + (size_t)s * KEYS_PER_WG + (size_t)(wave - 1) * PAGE + r) * DH + 8 * c;
    const bf16_t* kp = p.kc + base;
    const bf16_t* vp = p.vc + base;
    u32x4 Kb[NB][4], Vb[NB][4];
#define ISSUE(b_, i_) do { _Pragma("unroll") for (int t_ = 0; t_ < 4; ++t_) { \
    Kb[b_][t_] = __builtin_nontemporal_load(reinterpret_cast<const u32x4*>(kp + (size_t)(i_) * (SW * PAGE * DH) + t_ * 4 * DH)); \
    Vb[b_][t_] = __builtin_nontemporal_load(reinterpret_cast<const u32x4*>(vp + (size_t)(i_) * (SW * PAGE * DH) + t_ * 4 * DH)); } } while (0)
    ISSUE(0, 0);
    __builtin_amdgcn_sched_barrier(0);
    asm volatile("; streaming waves" : "+v"(ev), "+v"(xv), "+v"(xt), "+v"(wq0), "+v"(wq1) :: "memory");
    qpartial();
    tag = __builtin_amdgcn_readfirstlane(ev) + 1u;
    __syncthreads();
#define HEADMODE 3
    ISSUE(1, 1);
#pragma unroll
    for (int b = 2; b < NB; ++b) { asm volatile("s_waitcnt vmcnt(8)" ::: "memory"); ISSUE(b, b); }
    __syncthreads();
    u32x4 qpk[4];
#pragma unroll
    for (int h = 0; h < 4; ++h) qpk[h] = *reinterpret_cast<const u32x4*>(smem + L_Q + h * 256 + 16 * c);
    float m = -INFINITY, l = 0.f, acc[32];
#pragma unroll
    for (int k = 0; k < 32; ++k) acc[k] = 0.f;
#define PAGE_BODY(b) do { \
      float x[16]; \
      _Pragma("unroll") for (int t = 0; t < 4; ++t) \
        _Pragma("unroll") for (int h = 0; h < 4; ++h) { float d = 0.f; \
          _Pragma("unroll") for (int k = 0; k < 4; ++k) d = dot2(Kb[b][t][k], qpk[h][k], d); \
          x[t * 4 + h] = d; } \
      TR_STEP(x, 8, XOR8, 8) TR_STEP(x, 4, XOR7, 4) TR_STEP(x, 2, XOR2, 2) TR_STEP(x, 1, XOR1, 1) \
      const float sc = bfr(bfr(x[0]) * SCALE); \
      float pm = sc; pm = fmaxf(pm, dpp<ROR4>(pm)); pm = fmaxf(pm, dpp<XOR8>(pm)); pm = xrow16_max(pm); \
      if (__any(pm > m)) { \
        const float mn = fmaxf(m, pm); \
        const float alpha = __builtin_amdgcn_exp2f((m - mn) * LOG2E); \
        l *= alpha; m = mn; \
        const float a0 = dpp<0x00>(alpha), a1 = dpp<0x55>(alpha), a2 = dpp<0xAA>(alpha), a3 = dpp<0xFF>(alpha); \
        _Pragma("unroll") for (int k = 0; k < 8; ++k) { acc[k] *= a0; acc[8 + k] *= a1; acc[16 + k] *= a2; acc[24 + k] *= a3; } \
      } \
      const float pp = __builtin_amdgcn_exp2f((sc - m) * LOG2E); \
      l += pp; \
      _Pragma("unroll") for (int t = 0; t < 4; ++t) { \
        float vf[8]; \
        _Pragma("unroll") for (int k = 0; k < 4; ++k) { vf[2 * k] = bf2f(Vb[b][t][k]); vf[2 * k + 1] = bfhi(Vb[b][t][k]); } \
        float pb[4]; \
        if (t == 0) { pb[0] = dpp<0x150>(pp); pb[1] = dpp<0x151>(pp); pb[2] = dpp<0x152>(pp); pb[3] = dpp<0x153>(pp); } \
        if (t == 1) { pb[0] = dpp<0x154>(pp); pb[1] = dpp<0x155>(pp); pb[2] = dpp<0x156>(pp); pb[3] = dpp<0x157>(pp); } \
        if (t == 2) { pb[0] = dpp<0x158>(pp); pb[1] = dpp<0x159>(pp); pb[2] = dpp<0x15A>(pp); pb[3] = dpp<0x15B>(pp); } \
        if (t == 3) { pb[0] = dpp<0x15C>(pp); pb[1] = dpp<0x15D>(pp); pb[2] = dpp<0x15E>(pp); pb[3] = dpp<0x15F>(pp); } \
        _Pragma("unroll") for (int h = 0; h < 4; ++h) \
          _Pragma("unroll") for (int k = 0; k < 8; ++k) acc[h * 8 + k] = fmaf(pb[h], vf[k], acc[h * 8 + k]); \
      } } while (0)
    const bool extra = wave <= NPG % SW;
    constexpr int NLOOP = ((PFULL - NB) / NB) * NB;
    for (int i0 = 0; i0 < NLOOP; i0 += NB) {
#pragma unroll
      for (int b = 0; b < NB; ++b) {
        PAGE_BODY(b);
        ISSUE(b, i0 + b + NB);
      }
    }
#pragma unroll
    for (int j = 0; j < PFULL - NLOOP; ++j) {
      PAGE_BODY(j % NB);
      if (NLOOP + j + NB < PFULL || (NLOOP + j + NB == PFULL && extra)) ISSUE(j % NB, NLOOP + j + NB);
    }
    if (extra) PAGE_BODY((PFULL - NLOOP) % NB);
#undef PAGE_BODY
#undef ISSUE
    if (wave == 1) STAMP(3);
    if (wave == 7) STAMP(10);
#pragma unroll
    for (int k = 0; k < 32; ++k) acc[k] = xrow16_sum(acc[k]);
    { float t = l; t += dpp<ROR4>(t); t += dpp<XOR8>(t); l = xrow16_sum(t); }
    if (r == 0) {
#pragma unroll
      for (int h = 0; h < 4; ++h) {
        float* rec = mrg + ((wave - 1) * 4 + h) * 132;
        *reinterpret_cast<f32x4*>(rec + 4 + 8 * c) = f32x4{acc[h * 8], acc[h * 8 + 1], acc[h * 8 + 2], acc[h * 8 + 3]};
        *reinterpret_cast<f32x4*>(rec + 8 + 8 * c) = f32x4{acc[h * 8 + 4], acc[h * 8 + 5], acc[h * 8 + 6], acc[h * 8 + 7]};
        if (c == h) { rec[0] = m; rec[1] = l; }
      }
    }
    __syncthreads();
  }
  if (wave == 0) STAMP(4);
  if (tid < 288) {
    const int h = tid < 256 ? (tid >> 6) : ((tid - 256) >> 3);
    const int d = 2 * (tid & 63);
    float M = -INFINITY;
#pragma unroll
    for (int w = 0; w < SW; ++w) M = fmaxf(M, mrg[(w * 4 + h) * 132]);
    float L = 0.f, o0 = 0.f, o1 = 0.f;
#pragma unroll
    for (int w = 0; w < SW; ++w) { const float* rec = mrg + (w * 4 + h) * 132;
      const float wt = __builtin_amdgcn_exp2f((rec[0] - M) * LOG2E);
      L = fmaf(wt, rec[1], L); o0 = fmaf(wt, rec[4 + d], o0); o1 = fmaf(wt, rec[5 + d], o1); }
    if (tid < 256) GSTORE(WS_PA + (g * 32 + h * 8 + ((tid & 63) >> 3)) * 4608 + s * 128 + (tid & 7) * 16, f2u(o0), f2u(o1));
    else           GSTORE(WS_PA + (g * 32 + (tid - 256)) * 4608 + 4096 + s * 16, f2u(M), f2u(L));
  }
  float* const inb = reinterpret_cast<float*>(smem + L_IN);
  if (tid < 256) {
    const unsigned box = WS_PA + (g * 32 + s) * 4608;
    u32x4 a, b;
#pragma unroll 1
    for (int spin = 0; spin < SPIN_MAX; ++spin) {
      asm volatile("" ::: "memory");
      a = GLOAD(box + tid * 16); b = GLOAD(box + 4096 + (lane & 31) * 16);
      if (__all(a[0] == tag && a[2] == tag && b[0] == tag && b[2] == tag)) break;
      __builtin_amdgcn_s_sleep(2);
    }
    const float ms = u2f(b[1]), M = wave_max(ms);
    const float e = __builtin_amdgcn_exp2f((ms - M) * LOG2E);
    const float L = 0.5f * wave_sum(e * u2f(b[3]));
    const float wt = __shfl(e / L, tid >> 3);
    float o0 = wt * u2f(a[1]), o1 = wt * u2f(a[3]);
    o0 += dpp<XOR8>(o0); o1 += dpp<XOR8>(o1);
    o0 = xrow16_sum(o0); o1 = xrow16_sum(o1);
    if (lane < 8) { inb[(2 * lane) * 4 + wave] = o0; inb[(2 * lane + 1) * 4 + wave] = o1; }
  }
  __syncthreads();
  if (wave == 0) STAMP(5);
  if (tid < 256) {
    float a0 = 0.f, a1 = 0.f;
#pragma unroll
    for (int i = 0; i < 16; ++i) {
      const f32x4 q4 = *reinterpret_cast<const f32x4*>(inb + i * 4);
      const float oi = bfr((q4[0] + q4[1]) + (q4[2] + q4[3]));
      const unsigned w = *reinterpret_cast<const unsigned*>(smem + L_WO + i * 1024 + tid * 4);
      a0 = fmaf(oi, bf2f(w), a0); a1 = fmaf(oi, bfhi(w), a1);
    }
    GSTORE(WS_AP + ((tid >> 2) * 256 + wg) * 64 + (tid & 3) * 16, f2u(a0), f2u(a1));
  }
  const unsigned lbase = (unsigned)(uintptr_t)(LAS unsigned char*)smem;
  if (wg < RB0) {
    const int ju = wg - UP0;
    {
      const bf16_t* w1p = p.w1 + (size_t)(wave * 64 + (lane >> 3)) * FF + ju * 64 + (lane & 7) * 8;
#pragma unroll
      for (int i = 0; i < 8; ++i) glds16(w1p + (size_t)i * 8 * FF, lbase + L_W1 + wave * 8192 + i * 1024);
    }
    float* const hs = reinterpret_cast<float*>(smem + L_H);
    if (tid < 256) {
      u32x4 a;
#pragma unroll 1
      for (int spin = 0; spin < SPIN_MAX; ++spin) {
        asm volatile("" ::: "memory");
        a = GLOAD(WS_HG + tid * 16);
        if (__all(a[0] == tag && a[2] == tag)) break;
        __builtin_amdgcn_s_sleep(2);
      }
      hs[2 * tid] = u2f(a[1]); hs[2 * tid + 1] = u2f(a[3]);
    }
    __syncthreads();
    if (wave == 0) STAMP(7);
    float* const hn = reinterpret_cast<float*>(smem + L_U);
    {
      float hv[8];
#pragma unroll
      for (int k = 0; k < 8; ++k) hv[k] = hs[lane * 8 + k];
      float mm, dd; ln_stats8(hv, mm, dd);
      hn[tid] = bfr(bfr(hs[tid] - mm) / dd);
    }
    asm volatile("s_waitcnt vmcnt(0)" ::: "memory");
    __syncthreads();
    {
      float y[8] = {0, 0, 0, 0, 0, 0, 0, 0};
#pragma unroll
      for (int i = 0; i < 8; ++i) {
        const int row = (tid >> 3) + 64 * i;
        const u32x4 w = *reinterpret_cast<const u32x4*>(smem + L_W1 + row * 128 + (tid & 7) * 16);
        const float hvv = hn[row];
#pragma unroll
        for (int k = 0; k < 4; ++k) { y[2 * k] = fmaf(hvv, bf2f(w[k]), y[2 * k]); y[2 * k + 1] = fmaf(hvv, bfhi(w[k]), y[2 * k + 1]); }
      }
#pragma unroll
      for (int k = 0; k < 8; ++k) { y[k] += dpp<XOR8>(y[k]); y[k] = xrow16_sum(y[k]); }
      if (lane < 8) {
        *reinterpret_cast<f32x4*>(red + wave * 64 + lane * 8) = f32x4{y[0], y[1], y[2], y[3]};
        *reinterpret_cast<f32x4*>(red + wave * 64 + lane * 8 + 4) = f32x4{y[4], y[5], y[6], y[7]};
      }
    }
    __syncthreads();
    if (wave == 0) {
      float sum = 0.f;
#pragma unroll
      for (int w = 0; w < WAVES; ++w) sum += red[w * 64 + lane];
      const float hv = bfr(bfr(sum) + bf2f(p.b1[ju * 64 + lane]));
      const float rr = hv > 0.f ? hv : 0.f;
      const unsigned ub = f2bf(rr * rr);
      const int k4 = 4 * (lane & 15);
      const unsigned b0 = __shfl(ub, k4), b1 = __shfl(ub, k4 + 1), b2 = __shfl(ub, k4 + 2), b3 = __shfl(ub, k4 + 3);
      if (lane < 16) GSTORE(WS_UG + (ju * 16 + lane) * 16, b0 | (b1 << 16), b2 | (b3 << 16));
    }
  } else if (wg < DN0) {
    const int jb = wg - RB0;
    u32x4 a, b;
#pragma unroll 1
    for (int spin = 0; spin < SPIN_MAX; ++spin) {
      asm volatile("" ::: "memory");
      a = GLOAD(WS_AP + jb * 16384 + tid * 16); b = GLOAD(WS_AP + jb * 16384 + 8192 + tid * 16);
      if (__all(a[0] == tag && a[2] == tag && b[0] == tag && b[2] == tag)) break;
      __builtin_amdgcn_s_sleep(2);
    }
    float s0 = u2f(a[1]) + u2f(b[1]), s1 = u2f(a[3]) + u2f(b[3]);
    s0 += dpp<ROR4>(s0); s0 += dpp<XOR8>(s0); s0 = xrow16_sum(s0);
    s1 += dpp<ROR4>(s1); s1 += dpp<XOR8>(s1); s1 = xrow16_sum(s1);
    if (lane < 4) { red[wave * 8 + 2 * lane] = s0; red[wave * 8 + 2 * lane + 1] = s1; }
    __syncthreads();
    if (wave == 0) STAMP(6);
    if (tid < 4) {
      float t0 = 0.f, t1 = 0.f;
#pragma unroll
      for (int w = 0; w < WAVES; ++w) { t0 += red[w * 8 + 2 * tid]; t1 += red[w * 8 + 2 * tid + 1]; }
      const unsigned xx = *reinterpret_cast<const unsigned*>(p.x + jb * 8 + 2 * tid);
      const float h0 = bfr(bf2f(xx) + bfr(t0)), h1 = bfr(bfhi(xx) + bfr(t1));
      GSTORE(WS_HG + (jb * 4 + tid) * 16, f2u(h0), f2u(h1));
    }
  } else if (wg < DN0 + NDN) {
    const int jd = wg - DN0;
    {
      const bf16_t* w2p = p.w2 + (size_t)(wave * 224 + (lane >> 2)) * DM + jd * 32 + (lane & 3) * 8;
#pragma unroll
      for (int i = 0; i < 14; ++i) glds16(w2p + (size_t)i * 16 * DM, lbase + L_W2 + wave * 14336 + i * 1024);
    }
    float* const us = reinterpret_cast<float*>(smem + L_U);
    float* const hs = reinterpret_cast<float*>(smem + L_H);
    {
      const unsigned off = tid < 448 ? WS_UG + tid * 16 : WS_HG + (jd * 16 + (tid & 15)) * 16;
      u32x4 a;
#pragma unroll 1
      for (int spin = 0; spin < SPIN_MAX; ++spin) {
        asm volatile("" ::: "memory");
        a = GLOAD(off);
        if (__all(a[0] == tag && a[2] == tag)) break;
        __builtin_amdgcn_s_sleep(2);
      }
      if (tid < 448) *reinterpret_cast<f32x4*>(us + 4 * tid) = f32x4{bf2f(a[1]), bfhi(a[1]), bf2f(a[3]), bfhi(a[3])};
      else if (tid < 464) { hs[2 * (tid & 15)] = u2f(a[1]); hs[2 * (tid & 15) + 1] = u2f(a[3]); }
    }
    asm volatile("s_waitcnt vmcnt(0)" ::: "memory");
    __syncthreads();
    if (wave == 0) STAMP(8);
    {
      float y[8] = {0, 0, 0, 0, 0, 0, 0, 0};
#pragma unroll
      for (int i = 0; i < 14; ++i) {
        const int row = (tid >> 2) + 128 * i;
        const u32x4 w = *reinterpret_cast<const u32x4*>(smem + L_W2 + row * 64 + (tid & 3) * 16);
        const float uv = us[row];
#pragma unroll
        for (int k = 0; k < 4; ++k) { y[2 * k] = fmaf(uv, bf2f(w[k]), y[2 * k]); y[2 * k + 1] = fmaf(uv, bfhi(w[k]), y[2 * k + 1]); }
      }
#pragma unroll
      for (int k = 0; k < 8; ++k) { y[k] += dpp<ROR4>(y[k]); y[k] += dpp<XOR8>(y[k]); y[k] = xrow16_sum(y[k]); }
      if (lane < 4) {
        *reinterpret_cast<f32x4*>(red + wave * 32 + lane * 8) = f32x4{y[0], y[1], y[2], y[3]};
        *reinterpret_cast<f32x4*>(red + wave * 32 + lane * 8 + 4) = f32x4{y[4], y[5], y[6], y[7]};
      }
    }
    __syncthreads();
    if (tid < 32) {
      const int col = jd * 32 + tid;
      float sum = 0.f;
#pragma unroll
      for (int w = 0; w < WAVES; ++w) sum += red[w * 32 + tid];
      const float ov = bfr(bfr(sum) + bf2f(p.b2[col]));
      p.out[col] = (bf16_t)f2bf(hs[tid] + ov);
    }
    if (wave == 0) STAMP(9);
    if (wg == DN0 && tid == 0) __hip_atomic_store(reinterpret_cast<unsigned*>(p.ws + WS_EPOCH), tag, __ATOMIC_RELAXED, __HIP_MEMORY_SCOPE_AGENT);
  }
}

extern "C" void kernel_launch(void* const* d_in, const int* in_sizes, int n_in,
                              void* d_out, int out_size, void* d_ws, size_t ws_size,
                              hipStream_t stream) {
  Params p;
  p.x  = (const bf16_t*)d_in[0];
  p.kc = (const bf16_t*)d_in[1];
  p.vc = (const bf16_t*)d_in[2];
  p.wq = (const bf16_t*)d_in[3];
  p.wo = (const bf16_t*)d_in[4];
  p.w1 = (const bf16_t*)d_in[6];
  p.b1 = (const bf16_t*)d_in[7];
  p.w2 = (const bf16_t*)d_in[8];
  p.b2 = (const bf16_t*)d_in[9];
  p.out = (bf16_t*)d_out;
  p.ws = (unsigned char*)d_ws;
  fused_decode<<<NWG, NTHR, 0, stream>>>(p);
}
```

```cpp
#include <hip/hip_runtime.h>
#include <hip/hip_bf16.h>
#include <cstdint>

typedef unsigned short bf16_t;
using bf16x2 = __attribute__((ext_vector_type(2))) __bf16;
using u32x4  = __attribute__((ext_vector_type(4))) unsigned;
using u32x2  = __attribute__((ext_vector_type(2))) unsigned;
using f32x4  = __attribute__((ext_vector_type(4))) float;
#define LAS __attribute__((address_space(3)))

constexpr int DM = 512, HQ = 32, HKV = 8, SK = 65536, DH = 128, FF = 1792;
constexpr int NSPLIT = 32, NWG = HKV * NSPLIT, KEYS_PER_WG = SK / NSPLIT, PAGE = 16, WAVES = 8, NTHR = WAVES * 64;
constexpr int PPW = KEYS_PER_WG / PAGE / WAVES;
#define NBUF 3
constexpr int NB = NBUF;
constexpr int UP0 = 0, NUP = FF / 64, RB0 = UP0 + NUP, NRB = DM / 8, DN0 = RB0 + NRB, NDN = DM / 32;
constexpr float SCALE = 0.08838834764831845f;
constexpr float LOG2E = 1.4426950408889634f;
constexpr int SPIN_MAX = 1 << 18;

constexpr unsigned WS_EPOCH = 0;
constexpr unsigned WS_QG = 256;
constexpr unsigned WS_PA = WS_QG + 8 * 32 * 4 * 16;
constexpr unsigned WS_AP = WS_PA + 8 * 32 * 32 * 9 * 16;
constexpr unsigned WS_HG = WS_AP + 256 * 256 * 16;
constexpr unsigned WS_UG = WS_HG + 256 * 16;
constexpr unsigned WS_END = WS_UG + 112 * 4 * 16;

constexpr int L_WO = 0;
constexpr int L_W1 = L_WO + 16384;
constexpr int L_W2 = L_W1;
constexpr int L_MRG = L_W2 + 114688;
constexpr int L_Q = L_MRG + 8 * 4 * 132 * 4;
constexpr int L_RED = L_Q + 1024;
constexpr int L_H = L_RED + 2048;
constexpr int L_U = L_H + 2048;
constexpr int L_IN = L_U + 7168;
constexpr int L_END = L_IN + 32 * 18 * 4;

__device__ __forceinline__ float bf2f(unsigned b) { return __builtin_bit_cast(float, b << 16); }
__device__ __forceinline__ float bfhi(unsigned b) { return __builtin_bit_cast(float, b & 0xffff0000u); }
__device__ __forceinline__ float bfr(float x) { return (float)(__bf16)x; }
__device__ __forceinline__ unsigned f2bf(float x) { return (unsigned)__builtin_bit_cast(unsigned short, (__bf16)x); }
__device__ __forceinline__ float u2f(unsigned u) { return __builtin_bit_cast(float, u); }
__device__ __forceinline__ unsigned f2u(float f) { return __builtin_bit_cast(unsigned, f); }
__device__ __forceinline__ float dot2(unsigned k, unsigned q, float acc) {
  return __builtin_amdgcn_fdot2_f32_bf16(__builtin_bit_cast(bf16x2, k), __builtin_bit_cast(bf16x2, q), acc, false);
}
template <int CTRL> __device__ __forceinline__ float dpp(float x) {
  return __builtin_bit_cast(float, __builtin_amdgcn_mov_dpp(__builtin_bit_cast(int, x), CTRL, 0xf, 0xf, true));
}
constexpr int XOR1 = 0xB1, XOR2 = 0x4E, XOR7 = 0x141, XOR8 = 0x128, ROR4 = 0x124;
__device__ __forceinline__ float xrow16_max(float x) {
  auto s = __builtin_amdgcn_permlane16_swap(__float_as_uint(x), __float_as_uint(x), false, false);
  x = fmaxf(__uint_as_float(s[0]), __uint_as_float(s[1]));
  auto t = __builtin_amdgcn_permlane32_swap(__float_as_uint(x), __float_as_uint(x), false, false);
  return fmaxf(__uint_as_float(t[0]), __uint_as_float(t[1]));
}
__device__ __forceinline__ float xrow16_sum(float x) {
  auto s = __builtin_amdgcn_permlane16_swap(__float_as_uint(x), __float_as_uint(x), false, false);
  x = __uint_as_float(s[0]) + __uint_as_float(s[1]);
  auto t = __builtin_amdgcn_permlane32_swap(__float_as_uint(x), __float_as_uint(x), false, false);
  return __uint_as_float(t[0]) + __uint_as_float(t[1]);
}
__device__ __forceinline__ float wave_sum(float x) {
  x += dpp<XOR1>(x); x += dpp<XOR2>(x); x += dpp<XOR7>(x); x += dpp<XOR8>(x);
  return xrow16_sum(x);
}
__device__ __forceinline__ float wave_max(float x) {
  x = fmaxf(x, dpp<XOR1>(x)); x = fmaxf(x, dpp<XOR2>(x)); x = fmaxf(x, dpp<XOR7>(x)); x = fmaxf(x, dpp<XOR8>(x));
  return xrow16_max(x);
}
__device__ __forceinline__ void ln_stats8(const float (&v)[8], float& m_bf, float& d_bf) {
  float s = 0.f;
#pragma unroll
  for (int k = 0; k < 8; ++k) s += v[k];
  const float mean = wave_sum(s) * (1.f / DM);
  float q = 0.f;
#pragma unroll
  for (int k = 0; k < 8; ++k) { const float d = v[k] - mean; q = fmaf(d, d, q); }
  const float var = bfr(wave_sum(q) * (1.f / DM));
  m_bf = bfr(mean);
  d_bf = bfr(sqrtf(bfr(var + 1e-5f)));
}
__device__ __forceinline__ void glds16(const void* gsrc, unsigned lds_dst) {
  unsigned keep;
  asm volatile("s_mov_b32 %0, m0\n\ts_mov_b32 m0, %2\n\ts_nop 0\n\tglobal_load_lds_dwordx4 %1, off\n\ts_mov_b32 m0, %0"
               : "=&s"(keep) : "v"(gsrc), "s"(lds_dst) : "memory");
}

struct Params { const bf16_t *x, *kc, *vc, *wq, *wo, *w1, *b1, *w2, *b2; bf16_t* out; unsigned char* ws; };

#define STAMP(i_) do { } while (0)
#define GSTORE(off_, d0_, d1_) __builtin_amdgcn_raw_buffer_store_b128(u32x4{tag, (d0_), tag, (d1_)}, rsrc, (off_), 0, 16)
#define GLOAD(off_) __builtin_amdgcn_raw_buffer_load_b128(rsrc, (off_), 0, 16)

__global__ void __launch_bounds__(NTHR, 2) fused_decode(Params p) {
  __shared__ __attribute__((aligned(16))) unsigned char smem[L_END];
  const int wg = blockIdx.x, g = (wg >> 3) & 7, s = (wg & 7) * 4 + (wg >> 6);
  const int tid = threadIdx.x, lane = tid & 63, wave = __builtin_amdgcn_readfirstlane(tid >> 6), r = lane >> 4, c = lane & 15;
  const auto rsrc = __builtin_amdgcn_make_buffer_rsrc(p.ws, 0, WS_END, 0x00020000);
  float* const red = reinterpret_cast<float*>(smem + L_RED);

  if (wave == 0) STAMP(0);
  unsigned ev = __hip_atomic_load(reinterpret_cast<unsigned*>(p.ws + WS_EPOCH), __ATOMIC_RELAXED, __HIP_MEMORY_SCOPE_AGENT);
  u32x4 xv = *reinterpret_cast<const u32x4*>(p.x + 8 * lane);
  unsigned xt = p.x[tid];
  const bf16_t* wqp = p.wq + (size_t)tid * (HQ * DH) + g * 512 + s * 16;
  u32x4 wq0 = *reinterpret_cast<const u32x4*>(wqp), wq1 = *reinterpret_cast<const u32x4*>(wqp + 8);

  __builtin_amdgcn_s_barrier();
  constexpr int SW = WAVES - 1;
  constexpr int NPG = KEYS_PER_WG / PAGE;
  constexpr int PFULL = NPG / SW;
  float* const mrg = reinterpret_cast<float*>(smem + L_MRG);
#define TR_STEP(x, HALF, CTRL, BIT) _Pragma("unroll") for (int hh = 0; hh < (HALF); ++hh) { \
      const float lo_ = x[hh], hi_ = x[hh + (HALF)]; const float t1_ = lo_ + dpp<CTRL>(lo_), t2_ = hi_ + dpp<CTRL>(hi_); \
      x[hh] = (c & (BIT)) ? t2_ : t1_; }
  auto qpartial = [&]() {
    float xf[8];
#pragma unroll
    for (int k = 0; k < 4; ++k) { xf[2 * k] = bf2f(xv[k] & 0xffffu); xf[2 * k + 1] = bfhi(xv[k]); }
    float m, d; ln_stats8(xf, m, d);
    const float xn = bfr(bfr(bf2f(xt) - m) / d);
    float y[16];
#pragma unroll
    for (int k = 0; k < 4; ++k) { y[2 * k] = xn * bf2f(wq0[k] & 0xffffu); y[2 * k + 1] = xn * bfhi(wq0[k]);
                                  y[8 + 2 * k] = xn * bf2f(wq1[k] & 0xffffu); y[9 + 2 * k] = xn * bfhi(wq1[k]); }
    TR_STEP(y, 8, XOR8, 8) TR_STEP(y, 4, XOR7, 4) TR_STEP(y, 2, XOR2, 2) TR_STEP(y, 1, XOR1, 1)
    const float t = xrow16_sum(y[0]);
    if (r == 0) red[wave * 16 + c] = t;
  };
  unsigned tag;
  if (wave == 0) {
    asm volatile("; service wave" : "+v"(ev), "+v"(xv), "+v"(xt), "+v"(wq0), "+v"(wq1) :: "memory");
    qpartial();
    tag = __builtin_amdgcn_readfirstlane(ev) + 1u;
    __syncthreads();
    STAMP(1);
    float qs = 0.f;
#pragma unroll
    for (int w = 0; w < WAVES; ++w) qs += red[w * 16 + c];
    const unsigned qb = f2bf(qs);
    const unsigned b0 = __shfl(qb, 4 * (lane & 3)), b1 = __shfl(qb, 4 * (lane & 3) + 1), b2 = __shfl(qb, 4 * (lane & 3) + 2), b3 = __shfl(qb, 4 * (lane & 3) + 3);
    if (lane < 4) GSTORE(WS_QG + ((g * 32 + s) * 4 + lane) * 16, b0 | (b1 << 16), b2 | (b3 << 16));
    u32x4 a0, a1;
#pragma unroll 1
    for (int spin = 0; spin < SPIN_MAX; ++spin) {
      asm volatile("" ::: "memory");
      a0 = GLOAD(WS_QG + g * 2048 + lane * 16); a1 = GLOAD(WS_QG + g * 2048 + 1024 + lane * 16);
      const bool ok = a0[0] == tag && a0[2] == tag && a1[0] == tag && a1[2] == tag;
      if (__all(ok)) break;
      __builtin_amdgcn_s_sleep(1);
    }
    STAMP(2);
    *reinterpret_cast<u32x2*>(smem + L_Q + lane * 8) = u32x2{a0[1], a0[3]};
    *reinterpret_cast<u32x2*>(smem + L_Q + 512 + lane * 8) = u32x2{a1[1], a1[3]};
    __syncthreads();
    const unsigned lbase = (unsigned)(uintptr_t)(LAS unsigned char*)smem;
    const bf16_t* wop = p.wo + (size_t)(g * 512 + s * 16) * DM + 8 * lane;
#pragma unroll
    for (int i = 0; i < 16; ++i) glds16(wop + i * DM, lbase + L_WO + i * 1024);
    asm volatile("s_waitcnt vmcnt(0)" ::: "memory");
    __syncthreads();
  } else {
    const size_t base = ((size_t)g * SK + (size_t)s * KEYS_PER_WG + (size_t)(wave - 1) * PAGE + r) * DH + 8 * c;
    const bf16_t* kp = p.kc + base;
    const bf16_t* vp = p.vc + base;
    u32x4 Kb[NB][4], Vb[NB][4];
#define ISSUE(b_, i_) do { _Pragma("unroll") for (int t_ = 0; t_ < 4; ++t_) { \
    Kb[b_][t_] = __builtin_nontemporal_load(reinterpret_cast<const u32x4*>(kp + (size_t)(i_) * (SW * PAGE * DH) + t_ * 4 * DH)); \
    Vb[b_][t_] = __builtin_nontemporal_load(reinterpret_cast<const u32x4*>(vp + (size_t)(i_) * (SW * PAGE * DH) + t_ * 4 * DH)); } } while (0)
    ISSUE(0, 0);
    __builtin_amdgcn_sched_barrier(0);
    asm volatile("; streaming waves" : "+v"(ev), "+v"(xv), "+v"(xt), "+v"(wq0), "+v"(wq1) :: "memory");
    qpartial();
    tag = __builtin_amdgcn_readfirstlane(ev) + 1u;
    __syncthreads();
#define HEADMODE 4
    asm volatile("s_waitcnt vmcnt(0)" ::: "memory");
    ISSUE(1, 1);
#pragma unroll
    for (int b = 2; b < NB; ++b) { asm volatile("s_waitcnt vmcnt(0)" ::: "memory"); ISSUE(b, b); }
    __syncthreads();
    u32x4 qpk[4];
#pragma unroll
    for (int h = 0; h < 4; ++h) qpk[h] = *reinterpret_cast<const u32x4*>(smem + L_Q + h * 256 + 16 * c);
    float m = -INFINITY, l = 0.f, acc[32];
#pragma unroll
    for (int k = 0; k < 32; ++k) acc[k] = 0.f;
#define PAGE_BODY(b) do { \
      float x[16]; \
      _Pragma("unroll") for (int t = 0; t < 4; ++t) \
        _Pragma("unroll") for (int h = 0; h < 4; ++h) { float d = 0.f; \
          _Pragma("unroll") for (int k = 0; k < 4; ++k) d = dot2(Kb[b][t][k], qpk[h][k], d); \
          x[t * 4 + h] = d; } \
      TR_STEP(x, 8, XOR8, 8) TR_STEP(x, 4, XOR7, 4) TR_STEP(x, 2, XOR2, 2) TR_STEP(x, 1, XOR1, 1) \
      const float sc = bfr(bfr(x[0]) * SCALE); \
      float pm = sc; pm = fmaxf(pm, dpp<ROR4>(pm)); pm = fmaxf(pm, dpp<XOR8>(pm)); pm = xrow16_max(pm); \
      if (__any(pm > m)) { \
        const float mn = fmaxf(m, pm); \
        const float alpha = __builtin_amdgcn_exp2f((m - mn) * LOG2E); \
        l *= alpha; m = mn; \
        const float a0 = dpp<0x00>(alpha), a1 = dpp<0x55>(alpha), a2 = dpp<0xAA>(alpha), a3 = dpp<0xFF>(alpha); \
        _Pragma("unroll") for (int k = 0; k < 8; ++k) { acc[k] *= a0; acc[8 + k] *= a1; acc[16 + k] *= a2; acc[24 + k] *= a3; } \
      } \
      const float pp = __builtin_amdgcn_exp2f((sc - m) * LOG2E); \
      l += pp; \
      _Pragma("unroll") for (int t = 0; t < 4; ++t) { \
        float vf[8]; \
        _Pragma("unroll") for (int k = 0; k < 4; ++k) { vf[2 * k] = bf2f(Vb[b][t][k]); vf[2 * k + 1] = bfhi(Vb[b][t][k]); } \
        float pb[4]; \
        if (t == 0) { pb[0] = dpp<0x150>(pp); pb[1] = dpp<0x151>(pp); pb[2] = dpp<0x152>(pp); pb[3] = dpp<0x153>(pp); } \
        if (t == 1) { pb[0] = dpp<0x154>(pp); pb[1] = dpp<0x155>(pp); pb[2] = dpp<0x156>(pp); pb[3] = dpp<0x157>(pp); } \
        if (t == 2) { pb[0] = dpp<0x158>(pp); pb[1] = dpp<0x159>(pp); pb[2] = dpp<0x15A>(pp); pb[3] = dpp<0x15B>(pp); } \
        if (t == 3) { pb[0] = dpp<0x15C>(pp); pb[1] = dpp<0x15D>(pp); pb[2] = dpp<0x15E>(pp); pb[3] = dpp<0x15F>(pp); } \
        _Pragma("unroll") for (int h = 0; h < 4; ++h) \
          _Pragma("unroll") for (int k = 0; k < 8; ++k) acc[h * 8 + k] = fmaf(pb[h], vf[k], acc[h * 8 + k]); \
      } } while (0)
    const bool extra = wave <= NPG % SW;
    constexpr int NLOOP = ((PFULL - NB) / NB) * NB;
    for (int i0 = 0; i0 < NLOOP; i0 += NB) {
#pragma unroll
      for (int b = 0; b < NB; ++b) {
        PAGE_BODY(b);
        ISSUE(b, i0 + b + NB);
      }
    }
#pragma unroll
    for (int j = 0; j < PFULL - NLOOP; ++j) {
      PAGE_BODY(j % NB);
      if (NLOOP + j + NB < PFULL || (NLOOP + j + NB == PFULL && extra)) ISSUE(j % NB, NLOOP + j + NB);
    }
    if (extra) PAGE_BODY((PFULL - NLOOP) % NB);
#undef PAGE_BODY
#undef ISSUE
    if (wave == 1) STAMP(3);
    if (wave == 7) STAMP(10);
#pragma unroll
    for (int k = 0; k < 32; ++k) acc[k] = xrow16_sum(acc[k]);
    { float t = l; t += dpp<ROR4>(t); t += dpp<XOR8>(t); l = xrow16_sum(t); }
    if (r == 0) {
#pragma unroll
      for (int h = 0; h < 4; ++h) {
        float* rec = mrg + ((wave - 1) * 4 + h) * 132;
        *reinterpret_cast<f32x4*>(rec + 4 + 8 * c) = f32x4{acc[h * 8], acc[h * 8 + 1], acc[h * 8 + 2], acc[h * 8 + 3]};
        *reinterpret_cast<f32x4*>(rec + 8 + 8 * c) = f32x4{acc[h * 8 + 4], acc[h * 8 + 5], acc[h * 8 + 6], acc[h * 8 + 7]};
        if (c == h) { rec[0] = m; rec[1] = l; }
      }
    }
    __syncthreads();
  }
  if (wave == 0) STAMP(4);
  if (tid < 288) {
    const int h = tid < 256 ? (tid >> 6) : ((tid - 256) >> 3);
    const int d = 2 * (tid & 63);
    float M = -INFINITY;
#pragma unroll
    for (int w = 0; w < SW; ++w) M = fmaxf(M, mrg[(w * 4 + h) * 132]);
    float L = 0.f, o0 = 0.f, o1 = 0.f;
#pragma unroll
    for (int w = 0; w < SW; ++w) { const float* rec = mrg + (w * 4 + h) * 132;
      const float wt = __builtin_amdgcn_exp2f((rec[0] - M) * LOG2E);
      L = fmaf(wt, rec[1], L); o0 = fmaf(wt, rec[4 + d], o0); o1 = fmaf(wt, rec[5 + d], o1); }
    if (tid < 256) GSTORE(WS_PA + (g * 32 + h * 8 + ((tid & 63) >> 3)) * 4608 + s * 128 + (tid & 7) * 16, f2u(o0), f2u(o1));
    else           GSTORE(WS_PA + (g * 32 + (tid - 256)) * 4608 + 4096 + s * 16, f2u(M), f2u(L));
  }
  float* const inb = reinterpret_cast<float*>(smem + L_IN);
  if (tid < 256) {
    const unsigned box = WS_PA + (g * 32 + s) * 4608;
    u32x4 a, b;
#pragma unroll 1
    for (int spin = 0; spin < SPIN_MAX; ++spin) {
      asm volatile("" ::: "memory");
      a = GLOAD(box + tid * 16); b = GLOAD(box + 4096 + (lane & 31) * 16);
      if (__all(a[0] == tag && a[2] == tag && b[0] == tag && b[2] == tag)) break;
      __builtin_amdgcn_s_sleep(2);
    }
    const float ms = u2f(b[1]), M = wave_max(ms);
    const float e = __builtin_amdgcn_exp2f((ms - M) * LOG2E);
    const float L = 0.5f * wave_sum(e * u2f(b[3]));
    const float wt = __shfl(e / L, tid >> 3);
    float o0 = wt * u2f(a[1]), o1 = wt * u2f(a[3]);
    o0 += dpp<XOR8>(o0); o1 += dpp<XOR8>(o1);
    o0 = xrow16_sum(o0); o1 = xrow16_sum(o1);
    if (lane < 8) { inb[(2 * lane) * 4 + wave] = o0; inb[(2 * lane + 1) * 4 + wave] = o1; }
  }
  __syncthreads();
  if (wave == 0) STAMP(5);
  if (tid < 256) {
    float a0 = 0.f, a1 = 0.f;
#pragma unroll
    for (int i = 0; i < 16; ++i) {
      const f32x4 q4 = *reinterpret_cast<const f32x4*>(inb + i * 4);
      const float oi = bfr((q4[0] + q4[1]) + (q4[2] + q4[3]));
      const unsigned w = *reinterpret_cast<const unsigned*>(smem + L_WO + i * 1024 + tid * 4);
      a0 = fmaf(oi, bf2f(w), a0); a1 = fmaf(oi, bfhi(w), a1);
    }
    GSTORE(WS_AP + ((tid >> 2) * 256 + wg) * 64 + (tid & 3) * 16, f2u(a0), f2u(a1));
  }
  const unsigned lbase = (unsigned)(uintptr_t)(LAS unsigned char*)smem;
  if (wg < RB0) {
    const int ju = wg - UP0;
    const unsigned b1v = p.b1[ju * 64 + lane];
    {
      const bf16_t* w1p = p.w1 + (size_t)(wave * 64 + (lane >> 3)) * FF + ju * 64 + (lane & 7) * 8;
#pragma unroll
      for (int i = 0; i < 8; ++i) glds16(w1p + (size_t)i * 8 * FF, lbase + L_W1 + wave * 8192 + i * 1024);
    }
    float* const hs = reinterpret_cast<float*>(smem + L_H);
    if (tid < 256) {
      u32x4 a;
#pragma unroll 1
      for (int spin = 0; spin < SPIN_MAX; ++spin) {
        asm volatile("" ::: "memory");
        a = GLOAD(WS_HG + tid * 16);
        if (__all(a[0] == tag && a[2] == tag)) break;
        __builtin_amdgcn_s_sleep(2);
      }
      hs[2 * tid] = u2f(a[1]); hs[2 * tid + 1] = u2f(a[3]);
    }
    __syncthreads();
    if (wave == 0) STAMP(7);
    float* const hn = reinterpret_cast<float*>(smem + L_U);
    {
      float hv[8];
#pragma unroll
      for (int k = 0; k < 8; ++k) hv[k] = hs[lane * 8 + k];
      float mm, dd; ln_stats8(hv, mm, dd);
      hn[tid] = bfr(bfr(hs[tid] - mm) / dd);
    }
    asm volatile("s_waitcnt vmcnt(0)" ::: "memory");
    __syncthreads();
    {
      float y[8] = {0, 0, 0, 0, 0, 0, 0, 0};
#pragma unroll
      for (int i = 0; i < 8; ++i) {
        const int row = (tid >> 3) + 64 * i;
        const u32x4 w = *reinterpret_cast<const u32x4*>(smem + L_W1 + row * 128 + (tid & 7) * 16);
        const float hvv = hn[row];
#pragma unroll
        for (int k = 0; k < 4; ++k) { y[2 * k] = fmaf(hvv, bf2f(w[k]), y[2 * k]); y[2 * k + 1] = fmaf(hvv, bfhi(w[k]), y[2 * k + 1]); }
      }
#pragma unroll
      for (int k = 0; k < 8; ++k) { y[k] += dpp<XOR8>(y[k]); y[k] = xrow16_sum(y[k]); }
      if (lane < 8) {
        *reinterpret_cast<f32x4*>(red + wave * 64 + lane * 8) = f32x4{y[0], y[1], y[2], y[3]};
        *reinterpret_cast<f32x4*>(red + wave * 64 + lane * 8 + 4) = f32x4{y[4], y[5], y[6], y[7]};
      }
    }
    __syncthreads();
    if (wave == 0) {
      float sum = 0.f;
#pragma unroll
      for (int w = 0; w < WAVES; ++w) sum += red[w * 64 + lane];
      const float hv = bfr(bfr(sum) + bf2f(b1v));
      const float rr = hv > 0.f ? hv : 0.f;
      const unsigned ub = f2bf(rr * rr);
      const int k4 = 4 * (lane & 15);
      const unsigned b0 = __shfl(ub, k4), b1 = __shfl(ub, k4 + 1), b2 = __shfl(ub, k4 + 2), b3 = __shfl(ub, k4 + 3);
      if (lane < 16) GSTORE(WS_UG + (ju * 16 + lane) * 16, b0 | (b1 << 16), b2 | (b3 << 16));
    }
  } else if (wg < DN0) {
    const int jb = wg - RB0;
    const unsigned xx = *reinterpret_cast<const unsigned*>(p.x + jb * 8 + 2 * (tid & 3));
    u32x4 a, b;
#pragma unroll 1
    for (int spin = 0; spin < SPIN_MAX; ++spin) {
      asm volatile("" ::: "memory");
      a = GLOAD(WS_AP + jb * 16384 + tid * 16); b = GLOAD(WS_AP + jb * 16384 + 8192 + tid * 16);
      if (__all(a[0] == tag && a[2] == tag && b[0] == tag && b[2] == tag)) break;
      __builtin_amdgcn_s_sleep(2);
    }
    float s0 = u2f(a[1]) + u2f(b[1]), s1 = u2f(a[3]) + u2f(b[3]);
    s0 += dpp<ROR4>(s0); s0 += dpp<XOR8>(s0); s0 = xrow16_sum(s0);
    s1 += dpp<ROR4>(s1); s1 += dpp<XOR8>(s1); s1 = xrow16_sum(s1);
    if (lane < 4) { red[wave * 8 + 2 * lane] = s0; red[wave * 8 + 2 * lane + 1] = s1; }
    __syncthreads();
    if (wave == 0) STAMP(6);
    if (tid < 4) {
      float t0 = 0.f, t1 = 0.f;
#pragma unroll
      for (int w = 0; w < WAVES; ++w) { t0 += red[w * 8 + 2 * tid]; t1 += red[w * 8 + 2 * tid + 1]; }
      const float h0 = bfr(bf2f(xx) + bfr(t0)), h1 = bfr(bfhi(xx) + bfr(t1));
      GSTORE(WS_HG + (jb * 4 + tid) * 16, f2u(h0), f2u(h1));
    }
  } else if (wg < DN0 + NDN) {
    const int jd = wg - DN0;
    const unsigned b2v = p.b2[jd * 32 + (tid & 31)];
    {
      const bf16_t* w2p = p.w2 + (size_t)(wave * 224 + (lane >> 2)) * DM + jd * 32 + (lane & 3) * 8;
#pragma unroll
      for (int i = 0; i < 14; ++i) glds16(w2p + (size_t)i * 16 * DM, lbase + L_W2 + wave * 14336 + i * 1024);
    }
    float* const us = reinterpret_cast<float*>(smem + L_U);
    float* const hs = reinterpret_cast<float*>(smem + L_H);
    {
      const unsigned off = tid < 448 ? WS_UG + tid * 16 : WS_HG + (jd * 16 + (tid & 15)) * 16;
      u32x4 a;
#pragma unroll 1
      for (int spin = 0; spin < SPIN_MAX; ++spin) {
        asm volatile("" ::: "memory");
        a = GLOAD(off);
        if (__all(a[0] == tag && a[2] == tag)) break;
        __builtin_amdgcn_s_sleep(2);
      }
      if (tid < 448) *reinterpret_cast<f32x4*>(us + 4 * tid) = f32x4{bf2f(a[1]), bfhi(a[1]), bf2f(a[3]), bfhi(a[3])};
      else if (tid < 464) { hs[2 * (tid & 15)] = u2f(a[1]); hs[2 * (tid & 15) + 1] = u2f(a[3]); }
    }
    asm volatile("s_waitcnt vmcnt(0)" ::: "memory");
    __syncthreads();
    if (wave == 0) STAMP(8);
    {
      float y[8] = {0, 0, 0, 0, 0, 0, 0, 0};
#pragma unroll
      for (int i = 0; i < 14; ++i) {
        const int row = (tid >> 2) + 128 * i;
        const u32x4 w = *reinterpret_cast<const u32x4*>(smem + L_W2 + row * 64 + (tid & 3) * 16);
        const float uv = us[row];
#pragma unroll
        for (int k = 0; k < 4; ++k) { y[2 * k] = fmaf(uv, bf2f(w[k]), y[2 * k]); y[2 * k + 1] = fmaf(uv, bfhi(w[k]), y[2 * k + 1]); }
      }
#pragma unroll
      for (int k = 0; k < 8; ++k) { y[k] += dpp<ROR4>(y[k]); y[k] += dpp<XOR8>(y[k]); y[k] = xrow16_sum(y[k]); }
      if (lane < 4) {
        *reinterpret_cast<f32x4*>(red + wave * 32 + lane * 8) = f32x4{y[0], y[1], y[2], y[3]};
        *reinterpret_cast<f32x4*>(red + wave * 32 + lane * 8 + 4) = f32x4{y[4], y[5], y[6], y[7]};
      }
    }
    __syncthreads();
    if (tid < 32) {
      const int col = jd * 32 + tid;
      float sum = 0.f;
#pragma unroll
      for (int w = 0; w < WAVES; ++w) sum += red[w * 32 + tid];
      const float ov = bfr(bfr(sum) + bf2f(b2v));
      p.out[col] = (bf16_t)f2bf(hs[tid] + ov);
    }
    if (wave == 0) STAMP(9);
    if (wg == DN0 && tid == 0) __hip_atomic_store(reinterpret_cast<unsigned*>(p.ws + WS_EPOCH), tag, __ATOMIC_RELAXED, __HIP_MEMORY_SCOPE_AGENT);
  }
}

extern "C" void kernel_launch(void* const* d_in, const int* in_sizes, int n_in,
                              void* d_out, int out_size, void* d_ws, size_t ws_size,
                              hipStream_t stream) {
  Params p;
  p.x  = (const bf16_t*)d_in[0];
  p.kc = (const bf16_t*)d_in[1];
  p.vc = (const bf16_t*)d_in[2];
  p.wq = (const bf16_t*)d_in[3];
  p.wo = (const bf16_t*)d_in[4];
  p.w1 = (const bf16_t*)d_in[6];
  p.b1 = (const bf16_t*)d_in[7];
  p.w2 = (const bf16_t*)d_in[8];
  p.b2 = (const bf16_t*)d_in[9];
  p.out = (bf16_t*)d_out;
  p.ws = (unsigned char*)d_ws;
  fused_decode<<<NWG, NTHR, 0, stream>>>(p);
}
```

```cpp
#include <hip/hip_runtime.h>
#include <hip/hip_bf16.h>
#include <cstdint>

typedef unsigned short bf16_t;
using bf16x2 = __attribute__((ext_vector_type(2))) __bf16;
using u32x4  = __attribute__((ext_vector_type(4))) unsigned;
using u32x2  = __attribute__((ext_vector_type(2))) unsigned;
using f32x4  = __attribute__((ext_vector_type(4))) float;
#define LAS __attribute__((address_space(3)))

constexpr int DM = 512, HQ = 32, HKV = 8, SK = 65536, DH = 128, FF = 1792;
constexpr int NSPLIT = 32, NWG = HKV * NSPLIT, KEYS_PER_WG = SK / NSPLIT, PAGE = 16, WAVES = 8, NTHR = WAVES * 64;
constexpr int NB = 3;
constexpr int UP0 = 0, NUP = FF / 64, RB0 = UP0 + NUP, NRB = DM / 8, DN0 = RB0 + NRB, NDN = DM / 32;
constexpr float SCALE = 0.08838834764831845f;
constexpr float LOG2E = 1.4426950408889634f;
constexpr int SPIN_MAX = 1 << 18;

constexpr unsigned WS_EPOCH = 0;
constexpr unsigned WS_QG = 256;
constexpr unsigned WS_PA = WS_QG + 8 * 32 * 4 * 16;
constexpr unsigned WS_AP = WS_PA + 8 * 32 * 32 * 9 * 16;
constexpr unsigned WS_HG = WS_AP + 256 * 256 * 16;
constexpr unsigned WS_UG = WS_HG + 256 * 16;
constexpr unsigned WS_END = WS_UG + 112 * 4 * 16;

constexpr int L_WO = 0;
constexpr int L_W1 = L_WO + 16384;
constexpr int L_W2 = L_W1;
constexpr int L_MRG = L_W2 + 114688;
constexpr int L_Q = L_MRG + 8 * 4 * 132 * 4;
constexpr int L_RED = L_Q + 1024;
constexpr int L_H = L_RED + 2048;
constexpr int L_U = L_H + 2048;
constexpr int L_IN = L_U + 7168;
constexpr int L_END = L_IN + 32 * 18 * 4;

__device__ __forceinline__ float bf2f(unsigned b) { return __builtin_bit_cast(float, b << 16); }
__device__ __forceinline__ float bfhi(unsigned b) { return __builtin_bit_cast(float, b & 0xffff0000u); }
__device__ __forceinline__ float bfr(float x) { return (float)(__bf16)x; }
__device__ __forceinline__ unsigned f2bf(float x) { return (unsigned)__builtin_bit_cast(unsigned short, (__bf16)x); }
__device__ __forceinline__ float u2f(unsigned u) { return __builtin_bit_cast(float, u); }
__device__ __forceinline__ unsigned f2u(float f) { return __builtin_bit_cast(unsigned, f); }
__device__ __forceinline__ float dot2(unsigned k, unsigned q, float acc) {
  return __builtin_amdgcn_fdot2_f32_bf16(__builtin_bit_cast(bf16x2, k), __builtin_bit_cast(bf16x2, q), acc, false);
}
template <int CTRL> __device__ __forceinline__ float dpp(float x) {
  return __builtin_bit_cast(float, __builtin_amdgcn_mov_dpp(__builtin_bit_cast(int, x), CTRL, 0xf, 0xf, true));
}
constexpr int XOR1 = 0xB1, XOR2 = 0x4E, XOR7 = 0x141, XOR8 = 0x128, ROR4 = 0x124;
__device__ __forceinline__ float xrow16_max(float x) {
  auto s = __builtin_amdgcn_permlane16_swap(__float_as_uint(x), __float_as_uint(x), false, false);
  x = fmaxf(__uint_as_float(s[0]), __uint_as_float(s[1]));
  auto t = __builtin_amdgcn_permlane32_swap(__float_as_uint(x), __float_as_uint(x), false, false);
  return fmaxf(__uint_as_float(t[0]), __uint_as_float(t[1]));
}
__device__ __forceinline__ float xrow16_sum(float x) {
  auto s = __builtin_amdgcn_permlane16_swap(__float_as_uint(x), __float_as_uint(x), false, false);
  x = __uint_as_float(s[0]) + __uint_as_float(s[1]);
  auto t = __builtin_amdgcn_permlane32_swap(__float_as_uint(x), __float_as_uint(x), false, false);
  return __uint_as_float(t[0]) + __uint_as_float(t[1]);
}
__device__ __forceinline__ float wave_sum(float x) {
  x += dpp<XOR1>(x); x += dpp<XOR2>(x); x += dpp<XOR7>(x); x += dpp<XOR8>(x);
  return xrow16_sum(x);
}
__device__ __forceinline__ float wave_max(float x) {
  x = fmaxf(x, dpp<XOR1>(x)); x = fmaxf(x, dpp<XOR2>(x)); x = fmaxf(x, dpp<XOR7>(x)); x = fmaxf(x, dpp<XOR8>(x));
  return xrow16_max(x);
}
__device__ __forceinline__ void ln_stats8(const float (&v)[8], float& m_bf, float& d_bf) {
  float s = 0.f;
#pragma unroll
  for (int k = 0; k < 8; ++k) s += v[k];
  const float mean = wave_sum(s) * (1.f / DM);
  float q = 0.f;
#pragma unroll
  for (int k = 0; k < 8; ++k) { const float d = v[k] - mean; q = fmaf(d, d, q); }
  const float var = bfr(wave_sum(q) * (1.f / DM));
  m_bf = bfr(mean);
  d_bf = bfr(sqrtf(bfr(var + 1e-5f)));
}
__device__ __forceinline__ void glds16(const void* gsrc, unsigned lds_dst) {
  unsigned keep;
  asm volatile("s_mov_b32 %0, m0\n\ts_mov_b32 m0, %2\n\ts_nop 0\n\tglobal_load_lds_dwordx4 %1, off\n\ts_mov_b32 m0, %0"
               : "=&s"(keep) : "v"(gsrc), "s"(lds_dst) : "memory");
}

struct Params { const bf16_t *x, *kc, *vc, *wq, *wo, *w1, *b1, *w2, *b2; bf16_t* out; unsigned char* ws; };

#define GSTORE(off_, d0_, d1_) __builtin_amdgcn_raw_buffer_store_b128(u32x4{tag, (d0_), tag, (d1_)}, rsrc, (off_), 0, 16)
#define GLOAD(off_) __builtin_amdgcn_raw_buffer_load_b128(rsrc, (off_), 0, 16)

__global__ void __launch_bounds__(NTHR, 2) fused_decode(Params p) {
  __shared__ __attribute__((aligned(16))) unsigned char smem[L_END];
  const int wg = blockIdx.x, g = (wg >> 3) & 7, s = (wg & 7) * 4 + (wg >> 6);
  const int tid = threadIdx.x, lane = tid & 63, wave = __builtin_amdgcn_readfirstlane(tid >> 6), r = lane >> 4, c = lane & 15;
  const auto rsrc = __builtin_amdgcn_make_buffer_rsrc(p.ws, 0, WS_END, 0x00020000);
  float* const red = reinterpret_cast<float*>(smem + L_RED);

  unsigned ev = __hip_atomic_load(reinterpret_cast<unsigned*>(p.ws + WS_EPOCH), __ATOMIC_RELAXED, __HIP_MEMORY_SCOPE_AGENT);
  u32x4 xv = *reinterpret_cast<const u32x4*>(p.x + 8 * lane);
  unsigned xt = p.x[tid];
  const bf16_t* wqp = p.wq + (size_t)tid * (HQ * DH) + g * 512 + s * 16;
  u32x4 wq0 = *reinterpret_cast<const u32x4*>(wqp), wq1 = *reinterpret_cast<const u32x4*>(wqp + 8);

  __builtin_amdgcn_s_barrier();
  constexpr int SW = WAVES - 1;
  constexpr int NPG = KEYS_PER_WG / PAGE;
  constexpr int PFULL = NPG / SW;
  float* const mrg = reinterpret_cast<float*>(smem + L_MRG);
#define TR_STEP(x, HALF, CTRL, BIT) _Pragma("unroll") for (int hh = 0; hh < (HALF); ++hh) { \
      const float lo_ = x[hh], hi_ = x[hh + (HALF)]; const float t1_ = lo_ + dpp<CTRL>(lo_), t2_ = hi_ + dpp<CTRL>(hi_); \
      x[hh] = (c & (BIT)) ? t2_ : t1_; }
  auto qpartial = [&]() {
    float xf[8];
#pragma unroll
    for (int k = 0; k < 4; ++k) { xf[2 * k] = bf2f(xv[k] & 0xffffu); xf[2 * k + 1] = bfhi(xv[k]); }
    float m, d; ln_stats8(xf, m, d);
    const float xn = bfr(bfr(bf2f(xt) - m) / d);
    float y[16];
#pragma unroll
    for (int k = 0; k < 4; ++k) { y[2 * k] = xn * bf2f(wq0[k] & 0xffffu); y[2 * k + 1] = xn * bfhi(wq0[k]);
                                  y[8 + 2 * k] = xn * bf2f(wq1[k] & 0xffffu); y[9 + 2 * k] = xn * bfhi(wq1[k]); }
    TR_STEP(y, 8, XOR8, 8) TR_STEP(y, 4, XOR7, 4) TR_STEP(y, 2, XOR2, 2) TR_STEP(y, 1, XOR1, 1)
    const float t = xrow16_sum(y[0]);
    if (r == 0) red[wave * 16 + c] = t;
  };
  unsigned tag;
  if (wave == 0) {
    asm volatile("; service wave" : "+v"(ev), "+v"(xv), "+v"(xt), "+v"(wq0), "+v"(wq1) :: "memory");
    qpartial();
    tag = __builtin_amdgcn_readfirstlane(ev) + 1u;
    __syncthreads();
    float qs = 0.f;
#pragma unroll
    for (int w = 0; w < WAVES; ++w) qs += red[w * 16 + c];
    const unsigned qb = f2bf(qs);
    const unsigned b0 = __shfl(qb, 4 * (lane & 3)), b1 = __shfl(qb, 4 * (lane & 3) + 1), b2 = __shfl(qb, 4 * (lane & 3) + 2), b3 = __shfl(qb, 4 * (lane & 3) + 3);
    if (lane < 4) GSTORE(WS_QG + ((g * 32 + s) * 4 + lane) * 16, b0 | (b1 << 16), b2 | (b3 << 16));
    u32x4 a0, a1;
#pragma unroll 1
    for (int spin = 0; spin < SPIN_MAX; ++spin) {
      asm volatile("" ::: "memory");
      a0 = GLOAD(WS_QG + g * 2048 + lane * 16); a1 = GLOAD(WS_QG + g * 2048 + 1024 + lane * 16);
      const bool ok = a0[0] == tag && a0[2] == tag && a1[0] == tag && a1[2] == tag;
      if (__all(ok)) break;
      __builtin_amdgcn_s_sleep(1);
    }
    *reinterpret_cast<u32x2*>(smem + L_Q + lane * 8) = u32x2{a0[1], a0[3]};
    *reinterpret_cast<u32x2*>(smem + L_Q + 512 + lane * 8) = u32x2{a1[1], a1[3]};
    __syncthreads();
    const unsigned lbase = (unsigned)(uintptr_t)(LAS unsigned char*)smem;
    const bf16_t* wop = p.wo + (size_t)(g * 512 + s * 16) * DM + 8 * lane;
#pragma unroll
    for (int i = 0; i < 16; ++i) glds16(wop + i * DM, lbase + L_WO + i * 1024);
    asm volatile("s_waitcnt vmcnt(0)" ::: "memory");
    __syncthreads();
  } else {
    const size_t base = ((size_t)g * SK + (size_t)s * KEYS_PER_WG + (size_t)(wave - 1) * PAGE + r) * DH + 8 * c;
    const bf16_t* kp = p.kc + base;
    const bf16_t* vp = p.vc + base;
    u32x4 Kb[NB][4], Vb[NB][4];
#define ISSUE(b_, i_) do { _Pragma("unroll") for (int t_ = 0; t_ < 4; ++t_) { \
    Kb[b_][t_] = __builtin_nontemporal_load(reinterpret_cast<const u32x4*>(kp + (size_t)(i_) * (SW * PAGE * DH) + t_ * 4 * DH)); \
    Vb[b_][t_] = __builtin_nontemporal_load(reinterpret_cast<const u32x4*>(vp + (size_t)(i_) * (SW * PAGE * DH) + t_ * 4 * DH)); } } while (0)
    ISSUE(0, 0);
    __builtin_amdgcn_sched_barrier(0);
    asm volatile("; streaming waves" : "+v"(ev), "+v"(xv), "+v"(xt), "+v"(wq0), "+v"(wq1) :: "memory");
    qpartial();
    tag = __builtin_amdgcn_readfirstlane(ev) + 1u;
    __syncthreads();
#pragma unroll
    for (int b = 1; b < NB; ++b) { asm volatile("s_waitcnt vmcnt(0)" ::: "memory"); ISSUE(b, b); }
    __syncthreads();
    u32x4 qpk[4];
#pragma unroll
    for (int h = 0; h < 4; ++h) qpk[h] = *reinterpret_cast<const u32x4*>(smem + L_Q + h * 256 + 16 * c);
    float m = -INFINITY, l = 0.f, acc[32];
#pragma unroll
    for (int k = 0; k < 32; ++k) acc[k] = 0.f;
#define PAGE_BODY(b) do { \
      float x[16]; \
      _Pragma("unroll") for (int t = 0; t < 4; ++t) \
        _Pragma("unroll") for (int h = 0; h < 4; ++h) { float d = 0.f; \
          _Pragma("unroll") for (int k = 0; k < 4; ++k) d = dot2(Kb[b][t][k], qpk[h][k], d); \
          x[t * 4 + h] = d; } \
      TR_STEP(x, 8, XOR8, 8) TR_STEP(x, 4, XOR7, 4) TR_STEP(x, 2, XOR2, 2) TR_STEP(x, 1, XOR1, 1) \
      const float sc = bfr(bfr(x[0]) * SCALE); \
      float pm = sc; pm = fmaxf(pm, dpp<ROR4>(pm)); pm = fmaxf(pm, dpp<XOR8>(pm)); pm = xrow16_max(pm); \
      if (__any(pm > m)) { \
        const float mn = fmaxf(m, pm); \
        const float alpha = __builtin_amdgcn_exp2f((m - mn) * LOG2E); \
        l *= alpha; m = mn; \
        const float a0 = dpp<0x00>(alpha), a1 = dpp<0x55>(alpha), a2 = dpp<0xAA>(alpha), a3 = dpp<0xFF>(alpha); \
        _Pragma("unroll") for (int k = 0; k < 8; ++k) { acc[k] *= a0; acc[8 + k] *= a1; acc[16 + k] *= a2; acc[24 + k] *= a3; } \
      } \
      const float pp = __builtin_amdgcn_exp2f((sc - m) * LOG2E); \
      l += pp; \
      _Pragma("unroll") for (int t = 0; t < 4; ++t) { \
        float vf[8]; \
        _Pragma("unroll") for (int k = 0; k < 4; ++k) { vf[2 * k] = bf2f(Vb[b][t][k]); vf[2 * k + 1] = bfhi(Vb[b][t][k]); } \
        float pb[4]; \
        if (t == 0) { pb[0] = dpp<0x150>(pp); pb[1] = dpp<0x151>(pp); pb[2] = dpp<0x152>(pp); pb[3] = dpp<0x153>(pp); } \
        if (t == 1) { pb[0] = dpp<0x154>(pp); pb[1] = dpp<0x155>(pp); pb[2] = dpp<0x156>(pp); pb[3] = dpp<0x157>(pp); } \
        if (t == 2) { pb[0] = dpp<0x158>(pp); pb[1] = dpp<0x159>(pp); pb[2] = dpp<0x15A>(pp); pb[3] = dpp<0x15B>(pp); } \
        if (t == 3) { pb[0] = dpp<0x15C>(pp); pb[1] = dpp<0x15D>(pp); pb[2] = dpp<0x15E>(pp); pb[3] = dpp<0x15F>(pp); } \
        _Pragma("unroll") for (int h = 0; h < 4; ++h) \
          _Pragma("unroll") for (int k = 0; k < 8; ++k) acc[h * 8 + k] = fmaf(pb[h], vf[k], acc[h * 8 + k]); \
      } } while (0)
    const bool extra = wave <= NPG % SW;
    constexpr int NLOOP = ((PFULL - NB) / NB) * NB;
    for (int i0 = 0; i0 < NLOOP; i0 += NB) {
#pragma unroll
      for (int b = 0; b < NB; ++b) {
        PAGE_BODY(b);
        ISSUE(b, i0 + b + NB);
      }
    }
#pragma unroll
    for (int j = 0; j < PFULL - NLOOP; ++j) {
      PAGE_BODY(j % NB);
      if (NLOOP + j + NB < PFULL || (NLOOP + j + NB == PFULL && extra)) ISSUE(j % NB, NLOOP + j + NB);
    }
    if (extra) PAGE_BODY((PFULL - NLOOP) % NB);
#undef PAGE_BODY
#undef ISSUE
#pragma unroll
    for (int k = 0; k < 32; ++k) acc[k] = xrow16_sum(acc[k]);
    { float t = l; t += dpp<ROR4>(t); t += dpp<XOR8>(t); l = xrow16_sum(t); }
    if (r == 0) {
#pragma unroll
      for (int h = 0; h < 4; ++h) {
        float* rec = mrg + ((wave - 1) * 4 + h) * 132;
        *reinterpret_cast<f32x4*>(rec + 4 + 8 * c) = f32x4{acc[h * 8], acc[h * 8 + 1], acc[h * 8 + 2], acc[h * 8 + 3]};
        *reinterpret_cast<f32x4*>(rec + 8 + 8 * c) = f32x4{acc[h * 8 + 4], acc[h * 8 + 5], acc[h * 8 + 6], acc[h * 8 + 7]};
        if (c == h) { rec[0] = m; rec[1] = l; }
      }
    }
    __syncthreads();
  }
  if (tid < 288) {
    const int h = tid < 256 ? (tid >> 6) : ((tid - 256) >> 3);
    const int d = 2 * (tid & 63);
    float M = -INFINITY;
#pragma unroll
    for (int w = 0; w < SW; ++w) M = fmaxf(M, mrg[(w * 4 + h) * 132]);
    float L = 0.f, o0 = 0.f, o1 = 0.f;
#pragma unroll
    for (int w = 0; w < SW; ++w) { const float* rec = mrg + (w * 4 + h) * 132;
      const float wt = __builtin_amdgcn_exp2f((rec[0] - M) * LOG2E);
      L = fmaf(wt, rec[1], L); o0 = fmaf(wt, rec[4 + d], o0); o1 = fmaf(wt, rec[5 + d], o1); }
    if (tid < 256) GSTORE(WS_PA + (g * 32 + h * 8 + ((tid & 63) >> 3)) * 4608 + s * 128 + (tid & 7) * 16, f2u(o0), f2u(o1));
    else           GSTORE(WS_PA + (g * 32 + (tid - 256)) * 4608 + 4096 + s * 16, f2u(M), f2u(L));
  }
  float* const inb = reinterpret_cast<float*>(smem + L_IN);
  if (tid < 256) {
    const unsigned box = WS_PA + (g * 32 + s) * 4608;
    u32x4 a, b;
#pragma unroll 1
    for (int spin = 0; spin < SPIN_MAX; ++spin) {
      asm volatile("" ::: "memory");
      a = GLOAD(box + tid * 16); b = GLOAD(box + 4096 + (lane & 31) * 16);
      if (__all(a[0] == tag && a[2] == tag && b[0] == tag && b[2] == tag)) break;
      __builtin_amdgcn_s_sleep(2);
    }
    const float ms = u2f(b[1]), M = wave_max(ms);
    const float e = __builtin_amdgcn_exp2f((ms - M) * LOG2E);
    const float L = 0.5f * wave_sum(e * u2f(b[3]));
    const float wt = __shfl(e / L, tid >> 3);
    float o0 = wt * u2f(a[1]), o1 = wt * u2f(a[3]);
    o0 += dpp<XOR8>(o0); o1 += dpp<XOR8>(o1);
    o0 = xrow16_sum(o0); o1 = xrow16_sum(o1);
    if (lane < 8) { inb[(2 * lane) * 4 + wave] = o0; inb[(2 * lane + 1) * 4 + wave] = o1; }
  }
  __syncthreads();
  if (tid < 256) {
    float a0 = 0.f, a1 = 0.f;
#pragma unroll
    for (int i = 0; i < 16; ++i) {
      const f32x4 q4 = *reinterpret_cast<const f32x4*>(inb + i * 4);
      const float oi = bfr((q4[0] + q4[1]) + (q4[2] + q4[3]));
      const unsigned w = *reinterpret_cast<const unsigned*>(smem + L_WO + i * 1024 + tid * 4);
      a0 = fmaf(oi, bf2f(w), a0); a1 = fmaf(oi, bfhi(w), a1);
    }
    GSTORE(WS_AP + ((tid >> 2) * 256 + wg) * 64 + (tid & 3) * 16, f2u(a0), f2u(a1));
  }
  const unsigned lbase = (unsigned)(uintptr_t)(LAS unsigned char*)smem;
  if (wg < RB0) {
    const int ju = wg - UP0;
    const unsigned b1v = p.b1[ju * 64 + lane];
    {
      const bf16_t* w1p = p.w1 + (size_t)(wave * 64 + (lane >> 3)) * FF + ju * 64 + (lane & 7) * 8;
#pragma unroll
      for (int i = 0; i < 8; ++i) glds16(w1p + (size_t)i * 8 * FF, lbase + L_W1 + wave * 8192 + i * 1024);
    }
    float* const hs = reinterpret_cast<float*>(smem + L_H);
    if (tid < 256) {
      u32x4 a;
#pragma unroll 1
      for (int spin = 0; spin < SPIN_MAX; ++spin) {
        asm volatile("" ::: "memory");
        a = GLOAD(WS_HG + tid * 16);
        if (__all(a[0] == tag && a[2] == tag)) break;
        __builtin_amdgcn_s_sleep(2);
      }
      hs[2 * tid] = u2f(a[1]); hs[2 * tid + 1] = u2f(a[3]);
    }
    __syncthreads();
    float* const hn = reinterpret_cast<float*>(smem + L_U);
    {
      float hv[8];
#pragma unroll
      for (int k = 0; k < 8; ++k) hv[k] = hs[lane * 8 + k];
      float mm, dd; ln_stats8(hv, mm, dd);
      hn[tid] = bfr(bfr(hs[tid] - mm) / dd);
    }
    asm volatile("s_waitcnt vmcnt(0)" ::: "memory");
    __syncthreads();
    {
      float y[8] = {0, 0, 0, 0, 0, 0, 0, 0};
#pragma unroll
      for (int i = 0; i < 8; ++i) {
        const int row = (tid >> 3) + 64 * i;
        const u32x4 w = *reinterpret_cast<const u32x4*>(smem + L_W1 + row * 128 + (tid & 7) * 16);
        const float hvv = hn[row];
#pragma unroll
        for (int k = 0; k < 4; ++k) { y[2 * k] = fmaf(hvv, bf2f(w[k]), y[2 * k]); y[2 * k + 1] = fmaf(hvv, bfhi(w[k]), y[2 * k + 1]); }
      }
#pragma unroll
      for (int k = 0; k < 8; ++k) { y[k] += dpp<XOR8>(y[k]); y[k] = xrow16_sum(y[k]); }
      if (lane < 8) {
        *reinterpret_cast<f32x4*>(red + wave * 64 + lane * 8) = f32x4{y[0], y[1], y[2], y[3]};
        *reinterpret_cast<f32x4*>(red + wave * 64 + lane * 8 + 4) = f32x4{y[4], y[5], y[6], y[7]};
      }
    }
    __syncthreads();
    if (wave == 0) {
      float sum = 0.f;
#pragma unroll
      for (int w = 0; w < WAVES; ++w) sum += red[w * 64 + lane];
      const float hv = bfr(bfr(sum) + bf2f(b1v));
      const float rr = hv > 0.f ? hv : 0.f;
      const unsigned ub = f2bf(rr * rr);
      const int k4 = 4 * (lane & 15);
      const unsigned b0 = __shfl(ub, k4), b1 = __shfl(ub, k4 + 1), b2 = __shfl(ub, k4 + 2), b3 = __shfl(ub, k4 + 3);
      if (lane < 16) GSTORE(WS_UG + (ju * 16 + lane) * 16, b0 | (b1 << 16), b2 | (b3 << 16));
    }
  } else if (wg < DN0) {
    const int jb = wg - RB0;
    const unsigned xx = *reinterpret_cast<const unsigned*>(p.x + jb * 8 + 2 * (tid & 3));
    u32x4 a, b;
#pragma unroll 1
    for (int spin = 0; spin < SPIN_MAX; ++spin) {
      asm volatile("" ::: "memory");
      a = GLOAD(WS_AP + jb * 16384 + tid * 16); b = GLOAD(WS_AP + jb * 16384 + 8192 + tid * 16);
      if (__all(a[0] == tag && a[2] == tag && b[0] == tag && b[2] == tag)) break;
      __builtin_amdgcn_s_sleep(2);
    }
    float s0 = u2f(a[1]) + u2f(b[1]), s1 = u2f(a[3]) + u2f(b[3]);
    s0 += dpp<ROR4>(s0); s0 += dpp<XOR8>(s0); s0 = xrow16_sum(s0);
    s1 += dpp<ROR4>(s1); s1 += dpp<XOR8>(s1); s1 = xrow16_sum(s1);
    if (lane < 4) { red[wave * 8 + 2 * lane] = s0; red[wave * 8 + 2 * lane + 1] = s1; }
    __syncthreads();
    if (tid < 4) {
      float t0 = 0.f, t1 = 0.f;
#pragma unroll
      for (int w = 0; w < WAVES; ++w) { t0 += red[w * 8 + 2 * tid]; t1 += red[w * 8 + 2 * tid + 1]; }
      const float h0 = bfr(bf2f(xx) + bfr(t0)), h1 = bfr(bfhi(xx) + bfr(t1));
      GSTORE(WS_HG + (jb * 4 + tid) * 16, f2u(h0), f2u(h1));
    }
  } else if (wg < DN0 + NDN) {
    const int jd = wg - DN0;
    const unsigned b2v = p.b2[jd * 32 + (tid & 31)];
    {
      const bf16_t* w2p = p.w2 + (size_t)(wave * 224 + (lane >> 2)) * DM + jd * 32 + (lane & 3) * 8;
#pragma unroll
      for (int i = 0; i < 14; ++i) glds16(w2p + (size_t)i * 16 * DM, lbase + L_W2 + wave * 14336 + i * 1024);
    }
    float* const us = reinterpret_cast<float*>(smem + L_U);
    float* const hs = reinterpret_cast<float*>(smem + L_H);
    {
      const unsigned off = tid < 448 ? WS_UG + tid * 16 : WS_HG + (jd * 16 + (tid & 15)) * 16;
      u32x4 a;
#pragma unroll 1
      for (int spin = 0; spin < SPIN_MAX; ++spin) {
        asm volatile("" ::: "memory");
        a = GLOAD(off);
        if (__all(a[0] == tag && a[2] == tag)) break;
        __builtin_amdgcn_s_sleep(2);
      }
      if (tid < 448) *reinterpret_cast<f32x4*>(us + 4 * tid) = f32x4{bf2f(a[1]), bfhi(a[1]), bf2f(a[3]), bfhi(a[3])};
      else if (tid < 464) { hs[2 * (tid & 15)] = u2f(a[1]); hs[2 * (tid & 15) + 1] = u2f(a[3]); }
    }
    asm volatile("s_waitcnt vmcnt(0)" ::: "memory");
    __syncthreads();
    {
      float y[8] = {0, 0, 0, 0, 0, 0, 0, 0};
#pragma unroll
      for (int i = 0; i < 14; ++i) {
        const int row = (tid >> 2) + 128 * i;
        const u32x4 w = *reinterpret_cast<const u32x4*>(smem + L_W2 + row * 64 + (tid & 3) * 16);
        const float uv = us[row];
#pragma unroll
        for (int k = 0; k < 4; ++k) { y[2 * k] = fmaf(uv, bf2f(w[k]), y[2 * k]); y[2 * k + 1] = fmaf(uv, bfhi(w[k]), y[2 * k + 1]); }
      }
#pragma unroll
      for (int k = 0; k < 8; ++k) { y[k] += dpp<ROR4>(y[k]); y[k] += dpp<XOR8>(y[k]); y[k] = xrow16_sum(y[k]); }
      if (lane < 4) {
        *reinterpret_cast<f32x4*>(red + wave * 32 + lane * 8) = f32x4{y[0], y[1], y[2], y[3]};
        *reinterpret_cast<f32x4*>(red + wave * 32 + lane * 8 + 4) = f32x4{y[4], y[5], y[6], y[7]};
      }
    }
    __syncthreads();
    if (tid < 32) {
      const int col = jd * 32 + tid;
      float sum = 0.f;
#pragma unroll
      for (int w = 0; w < WAVES; ++w) sum += red[w * 32 + tid];
      const float ov = bfr(bfr(sum) + bf2f(b2v));
      p.out[col] = (bf16_t)f2bf(hs[tid] + ov);
    }
    if (wg == DN0 && tid == 0) __hip_atomic_store(reinterpret_cast<unsigned*>(p.ws + WS_EPOCH), tag, __ATOMIC_RELAXED, __HIP_MEMORY_SCOPE_AGENT);
  }
}

extern "C" void kernel_launch(void* const* d_in, const int* in_sizes, int n_in,
                              void* d_out, int out_size, void* d_ws, size_t ws_size,
                              hipStream_t stream) {
  Params p;
  p.x  = (const bf16_t*)d_in[0];
  p.kc = (const bf16_t*)d_in[1];
  p.vc = (const bf16_t*)d_in[2];
  p.wq = (const bf16_t*)d_in[3];
  p.wo = (const bf16_t*)d_in[4];
  p.w1 = (const bf16_t*)d_in[6];
  p.b1 = (const bf16_t*)d_in[7];
  p.w2 = (const bf16_t*)d_in[8];
  p.b2 = (const bf16_t*)d_in[9];
  p.out = (bf16_t*)d_out;
  p.ws = (unsigned char*)d_ws;
  fused_decode<<<NWG, NTHR, 0, stream>>>(p);
}
```

```cpp
#include <hip/hip_runtime.h>
#include <hip/hip_bf16.h>
#include <cstdint>

typedef unsigned short bf16_t;
using bf16x2 = __attribute__((ext_vector_type(2))) __bf16;
using u32x4  = __attribute__((ext_vector_type(4))) unsigned;
using u32x2  = __attribute__((ext_vector_type(2))) unsigned;
using f32x4  = __attribute__((ext_vector_type(4))) float;
#define LAS __attribute__((address_space(3)))

constexpr int DM = 512, HQ = 32, HKV = 8, SK = 65536, DH = 128, FF = 1792;
constexpr int NSPLIT = 32, NWG = HKV * NSPLIT, KEYS_PER_WG = SK / NSPLIT, PAGE = 16, WAVES = 8, NTHR = WAVES * 64;
constexpr int NB = 3;
constexpr int UP0 = 0, NUP = FF / 64, RB0 = UP0 + NUP, NRB = DM / 8;
constexpr float SCALE = 0.08838834764831845f;
constexpr float LOG2E = 1.4426950408889634f;
constexpr int SPIN_MAX = 1 << 18;

constexpr unsigned WS_EPOCH = 0;
constexpr unsigned WS_QG = 256;
constexpr unsigned WS_PA = WS_QG + 8 * 32 * 4 * 16;
constexpr unsigned WS_AP = WS_PA + 8 * 32 * 32 * 9 * 16;
constexpr unsigned WS_HG = WS_AP + 256 * 256 * 16;
constexpr unsigned WS_OP = WS_HG + 256 * 16;
constexpr unsigned WS_END = WS_OP + 64 * 56 * 4 * 16;

constexpr int L_W1 = 0;
constexpr int L_W2 = 65536;
constexpr int L_WO = L_W2;
constexpr int L_MRG = L_WO + 16384;
constexpr int L_Q = L_W2 + 65536;
constexpr int L_RED = L_Q + 1024;
constexpr int L_H = L_RED + 2048;
constexpr int L_U = L_H + 2048;
constexpr int L_IN = L_U + 2048 + 256;
constexpr int L_END = L_IN + 256;

__device__ __forceinline__ float bf2f(unsigned b) { return __builtin_bit_cast(float, b << 16); }
__device__ __forceinline__ float bfhi(unsigned b) { return __builtin_bit_cast(float, b & 0xffff0000u); }
__device__ __forceinline__ float bfr(float x) { return (float)(__bf16)x; }
__device__ __forceinline__ unsigned f2bf(float x) { return (unsigned)__builtin_bit_cast(unsigned short, (__bf16)x); }
__device__ __forceinline__ float u2f(unsigned u) { return __builtin_bit_cast(float, u); }
__device__ __forceinline__ unsigned f2u(float f) { return __builtin_bit_cast(unsigned, f); }
__device__ __forceinline__ float dot2(unsigned k, unsigned q, float acc) {
  return __builtin_amdgcn_fdot2_f32_bf16(__builtin_bit_cast(bf16x2, k), __builtin_bit_cast(bf16x2, q), acc, false);
}
template <int CTRL> __device__ __forceinline__ float dpp(float x) {
  return __builtin_bit_cast(float, __builtin_amdgcn_mov_dpp(__builtin_bit_cast(int, x), CTRL, 0xf, 0xf, true));
}
constexpr int XOR1 = 0xB1, XOR2 = 0x4E, XOR7 = 0x141, XOR8 = 0x128, ROR4 = 0x124;
__device__ __forceinline__ float xrow16_max(float x) {
  auto s = __builtin_amdgcn_permlane16_swap(__float_as_uint(x), __float_as_uint(x), false, false);
  x = fmaxf(__uint_as_float(s[0]), __uint_as_float(s[1]));
  auto t = __builtin_amdgcn_permlane32_swap(__float_as_uint(x), __float_as_uint(x), false, false);
  return fmaxf(__uint_as_float(t[0]), __uint_as_float(t[1]));
}
__device__ __forceinline__ float xrow16_sum(float x) {
  auto s = __builtin_amdgcn_permlane16_swap(__float_as_uint(x), __float_as_uint(x), false, false);
  x = __uint_as_float(s[0]) + __uint_as_float(s[1]);
  auto t = __builtin_amdgcn_permlane32_swap(__float_as_uint(x), __float_as_uint(x), false, false);
  return __uint_as_float(t[0]) + __uint_as_float(t[1]);
}
__device__ __forceinline__ float wave_sum(float x) {
  x += dpp<XOR1>(x); x += dpp<XOR2>(x); x += dpp<XOR7>(x); x += dpp<XOR8>(x);
  return xrow16_sum(x);
}
__device__ __forceinline__ float wave_max(float x) {
  x = fmaxf(x, dpp<XOR1>(x)); x = fmaxf(x, dpp<XOR2>(x)); x = fmaxf(x, dpp<XOR7>(x)); x = fmaxf(x, dpp<XOR8>(x));
  return xrow16_max(x);
}
__device__ __forceinline__ void ln_stats8(const float (&v)[8], float& m_bf, float& d_bf) {
  float s = 0.f;
#pragma unroll
  for (int k = 0; k < 8; ++k) s += v[k];
  const float mean = wave_sum(s) * (1.f / DM);
  float q = 0.f;
#pragma unroll
  for (int k = 0; k < 8; ++k) { const float d = v[k] - mean; q = fmaf(d, d, q); }
  const float var = bfr(wave_sum(q) * (1.f / DM));
  m_bf = bfr(mean);
  d_bf = bfr(sqrtf(bfr(var + 1e-5f)));
}
__device__ __forceinline__ void glds16(const void* gsrc, unsigned lds_dst) {
  unsigned keep;
  asm volatile("s_mov_b32 %0, m0\n\ts_mov_b32 m0, %2\n\ts_nop 0\n\tglobal_load_lds_dwordx4 %1, off nt\n\ts_mov_b32 m0, %0"
               : "=&s"(keep) : "v"(gsrc), "s"(lds_dst) : "memory");
}

struct Params { const bf16_t *x, *kc, *vc, *wq, *wo, *w1, *b1, *w2, *b2; bf16_t* out; unsigned char* ws; };

#define GSTORE(off_, d0_, d1_) __builtin_amdgcn_raw_buffer_store_b128(u32x4{tag, (d0_), tag, (d1_)}, rsrc, (off_), 0, 16)
#define GLOAD(off_) __builtin_amdgcn_raw_buffer_load_b128(rsrc, (off_), 0, 16)

__global__ void __launch_bounds__(NTHR, 2) fused_decode(Params p) {
  __shared__ __attribute__((aligned(16))) unsigned char smem[L_END];
  const int wg = blockIdx.x, g = (wg >> 3) & 7, s = (wg & 7) * 4 + (wg >> 6);
  const int tid = threadIdx.x, lane = tid & 63, wave = __builtin_amdgcn_readfirstlane(tid >> 6), r = lane >> 4, c = lane & 15;
  const auto rsrc = __builtin_amdgcn_make_buffer_rsrc(p.ws, 0, WS_END, 0x00020000);
  float* const red = reinterpret_cast<float*>(smem + L_RED);

  unsigned ev = __hip_atomic_load(reinterpret_cast<unsigned*>(p.ws + WS_EPOCH), __ATOMIC_RELAXED, __HIP_MEMORY_SCOPE_AGENT);
  u32x4 xv = *reinterpret_cast<const u32x4*>(p.x + 8 * lane);
  unsigned xt = p.x[tid];
  const bf16_t* wqp = p.wq + (size_t)tid * (HQ * DH) + g * 512 + s * 16;
  u32x4 wq0 = __builtin_nontemporal_load(reinterpret_cast<const u32x4*>(wqp)), wq1 = __builtin_nontemporal_load(reinterpret_cast<const u32x4*>(wqp + 8));

  __builtin_amdgcn_s_barrier();
  constexpr int SW = WAVES - 1;
  constexpr int NPG = KEYS_PER_WG / PAGE;
  constexpr int PFULL = NPG / SW;
  float* const mrg = reinterpret_cast<float*>(smem + L_MRG);
#define TR_STEP(x, HALF, CTRL, BIT) _Pragma("unroll") for (int hh = 0; hh < (HALF); ++hh) { \
      const float lo_ = x[hh], hi_ = x[hh + (HALF)]; const float t1_ = lo_ + dpp<CTRL>(lo_), t2_ = hi_ + dpp<CTRL>(hi_); \
      x[hh] = (c & (BIT)) ? t2_ : t1_; }
  auto qpartial = [&]() {
    float xf[8];
#pragma unroll
    for (int k = 0; k < 4; ++k) { xf[2 * k] = bf2f(xv[k] & 0xffffu); xf[2 * k + 1] = bfhi(xv[k]); }
    float m, d; ln_stats8(xf, m, d);
    const float xn = bfr(bfr(bf2f(xt) - m) / d);
    float y[16];
#pragma unroll
    for (int k = 0; k < 4; ++k) { y[2 * k] = xn * bf2f(wq0[k] & 0xffffu); y[2 * k + 1] = xn * bfhi(wq0[k]);
                                  y[8 + 2 * k] = xn * bf2f(wq1[k] & 0xffffu); y[9 + 2 * k] = xn * bfhi(wq1[k]); }
    TR_STEP(y, 8, XOR8, 8) TR_STEP(y, 4, XOR7, 4) TR_STEP(y, 2, XOR2, 2) TR_STEP(y, 1, XOR1, 1)
    const float t = xrow16_sum(y[0]);
    if (r == 0) red[wave * 16 + c] = t;
  };
  unsigned tag;
  if (wave == 0) {
    asm volatile("; service wave" : "+v"(ev), "+v"(xv), "+v"(xt), "+v"(wq0), "+v"(wq1) :: "memory");
    qpartial();
    tag = __builtin_amdgcn_readfirstlane(ev) + 1u;
    __syncthreads();
    float qs = 0.f;
#pragma unroll
    for (int w = 0; w < WAVES; ++w) qs += red[w * 16 + c];
    const unsigned qb = f2bf(qs);
    const unsigned b0 = __shfl(qb, 4 * (lane & 3)), b1 = __shfl(qb, 4 * (lane & 3) + 1), b2 = __shfl(qb, 4 * (lane & 3) + 2), b3 = __shfl(qb, 4 * (lane & 3) + 3);
    if (lane < 4) GSTORE(WS_QG + ((g * 32 + s) * 4 + lane) * 16, b0 | (b1 << 16), b2 | (b3 << 16));
    u32x4 a0, a1;
#pragma unroll 1
    for (int spin = 0; spin < SPIN_MAX; ++spin) {
      asm volatile("" ::: "memory");
      a0 = GLOAD(WS_QG + g * 2048 + lane * 16); a1 = GLOAD(WS_QG + g * 2048 + 1024 + lane * 16);
      const bool ok = a0[0] == tag && a0[2] == tag && a1[0] == tag && a1[2] == tag;
      if (__all(ok)) break;
      __builtin_amdgcn_s_sleep(1);
    }
    *reinterpret_cast<u32x2*>(smem + L_Q + lane * 8) = u32x2{a0[1], a0[3]};
    *reinterpret_cast<u32x2*>(smem + L_Q + 512 + lane * 8) = u32x2{a1[1], a1[3]};
    __syncthreads();
    const unsigned lbase = (unsigned)(uintptr_t)(LAS unsigned char*)smem;
    const bf16_t* wop = p.wo + (size_t)(g * 512 + s * 16) * DM + 8 * lane;
#pragma unroll
    for (int i = 0; i < 16; ++i) glds16(wop + i * DM, lbase + L_WO + i * 1024);
    asm volatile("s_waitcnt vmcnt(0)" ::: "memory");
    __syncthreads();
  } else {
    const size_t base = ((size_t)g * SK + (size_t)s * KEYS_PER_WG + (size_t)(wave - 1) * PAGE + r) * DH + 8 * c;
    const bf16_t* kp = p.kc + base;
    const bf16_t* vp = p.vc + base;
    u32x4 Kb[NB][4], Vb[NB][4];
#define ISSUE(b_, i_) do { _Pragma("unroll") for (int t_ = 0; t_ < 4; ++t_) { \
    Kb[b_][t_] = __builtin_nontemporal_load(reinterpret_cast<const u32x4*>(kp + (size_t)(i_) * (SW * PAGE * DH) + t_ * 4 * DH)); \
    Vb[b_][t_] = __builtin_nontemporal_load(reinterpret_cast<const u32x4*>(vp + (size_t)(i_) * (SW * PAGE * DH) + t_ * 4 * DH)); } } while (0)
    ISSUE(0, 0);
    __builtin_amdgcn_sched_barrier(0);
    asm volatile("; streaming waves" : "+v"(ev), "+v"(xv), "+v"(xt), "+v"(wq0), "+v"(wq1) :: "memory");
    qpartial();
    tag = __builtin_amdgcn_readfirstlane(ev) + 1u;
    __syncthreads();
#pragma unroll
    for (int b = 1; b < NB; ++b) { asm volatile("s_waitcnt vmcnt(0)" ::: "memory"); ISSUE(b, b); }
    __syncthreads();
    u32x4 qpk[4];
#pragma unroll
    for (int h = 0; h < 4; ++h) qpk[h] = *reinterpret_cast<const u32x4*>(smem + L_Q + h * 256 + 16 * c);
    float m = -INFINITY, l = 0.f, acc[32];
#pragma unroll
    for (int k = 0; k < 32; ++k) acc[k] = 0.f;
#define PAGE_BODY(b) do { \
      float x[16]; \
      _Pragma("unroll") for (int t = 0; t < 4; ++t) \
        _Pragma("unroll") for (int h = 0; h < 4; ++h) { float d = 0.f; \
          _Pragma("unroll") for (int k = 0; k < 4; ++k) d = dot2(Kb[b][t][k], qpk[h][k], d); \
          x[t * 4 + h] = d; } \
      TR_STEP(x, 8, XOR8, 8) TR_STEP(x, 4, XOR7, 4) TR_STEP(x, 2, XOR2, 2) TR_STEP(x, 1, XOR1, 1) \
      const float sc = bfr(bfr(x[0]) * SCALE); \
      float pm = sc; pm = fmaxf(pm, dpp<ROR4>(pm)); pm = fmaxf(pm, dpp<XOR8>(pm)); pm = xrow16_max(pm); \
      if (__any(pm > m)) { \
        const float mn = fmaxf(m, pm); \
        const float alpha = __builtin_amdgcn_exp2f((m - mn) * LOG2E); \
        l *= alpha; m = mn; \
        const float a0 = dpp<0x00>(alpha), a1 = dpp<0x55>(alpha), a2 = dpp<0xAA>(alpha), a3 = dpp<0xFF>(alpha); \
        _Pragma("unroll") for (int k = 0; k < 8; ++k) { acc[k] *= a0; acc[8 + k] *= a1; acc[16 + k] *= a2; acc[24 + k] *= a3; } \
      } \
      const float pp = __builtin_amdgcn_exp2f((sc - m) * LOG2E); \
      l += pp; \
      _Pragma("unroll") for (int t = 0; t < 4; ++t) { \
        float vf[8]; \
        _Pragma("unroll") for (int k = 0; k < 4; ++k) { vf[2 * k] = bf2f(Vb[b][t][k]); vf[2 * k + 1] = bfhi(Vb[b][t][k]); } \
        float pb[4]; \
        if (t == 0) { pb[0] = dpp<0x150>(pp); pb[1] = dpp<0x151>(pp); pb[2] = dpp<0x152>(pp); pb[3] = dpp<0x153>(pp); } \
        if (t == 1) { pb[0] = dpp<0x154>(pp); pb[1] = dpp<0x155>(pp); pb[2] = dpp<0x156>(pp); pb[3] = dpp<0x157>(pp); } \
        if (t == 2) { pb[0] = dpp<0x158>(pp); pb[1] = dpp<0x159>(pp); pb[2] = dpp<0x15A>(pp); pb[3] = dpp<0x15B>(pp); } \
        if (t == 3) { pb[0] = dpp<0x15C>(pp); pb[1] = dpp<0x15D>(pp); pb[2] = dpp<0x15E>(pp); pb[3] = dpp<0x15F>(pp); } \
        _Pragma("unroll") for (int h = 0; h < 4; ++h) \
          _Pragma("unroll") for (int k = 0; k < 8; ++k) acc[h * 8 + k] = fmaf(pb[h], vf[k], acc[h * 8 + k]); \
      } } while (0)
    const bool extra = wave <= NPG % SW;
    constexpr int NLOOP = ((PFULL - NB) / NB) * NB;
    for (int i0 = 0; i0 < NLOOP; i0 += NB) {
#pragma unroll
      for (int b = 0; b < NB; ++b) {
        PAGE_BODY(b);
        ISSUE(b, i0 + b + NB);
      }
    }
#pragma unroll
    for (int j = 0; j < PFULL - NLOOP; ++j) {
      PAGE_BODY(j % NB);
      if (NLOOP + j + NB < PFULL || (NLOOP + j + NB == PFULL && extra)) ISSUE(j % NB, NLOOP + j + NB);
    }
    if (extra) PAGE_BODY((PFULL - NLOOP) % NB);
#undef PAGE_BODY
#undef ISSUE
#pragma unroll
    for (int k = 0; k < 32; ++k) acc[k] = xrow16_sum(acc[k]);
    { float t = l; t += dpp<ROR4>(t); t += dpp<XOR8>(t); l = xrow16_sum(t); }
    if (r == 0) {
#pragma unroll
      for (int h = 0; h < 4; ++h) {
        float* rec = mrg + ((wave - 1) * 4 + h) * 132;
        *reinterpret_cast<f32x4*>(rec + 4 + 8 * c) = f32x4{acc[h * 8], acc[h * 8 + 1], acc[h * 8 + 2], acc[h * 8 + 3]};
        *reinterpret_cast<f32x4*>(rec + 8 + 8 * c) = f32x4{acc[h * 8 + 4], acc[h * 8 + 5], acc[h * 8 + 6], acc[h * 8 + 7]};
        if (c == h) { rec[0] = m; rec[1] = l; }
      }
    }
    __syncthreads();
  }
  if (tid < 288) {
    const int h = tid < 256 ? (tid >> 6) : ((tid - 256) >> 3);
    const int d = 2 * (tid & 63);
    float M = -INFINITY;
#pragma unroll
    for (int w = 0; w < SW; ++w) M = fmaxf(M, mrg[(w * 4 + h) * 132]);
    float L = 0.f, o0 = 0.f, o1 = 0.f;
#pragma unroll
    for (int w = 0; w < SW; ++w) { const float* rec = mrg + (w * 4 + h) * 132;
      const float wt = __builtin_amdgcn_exp2f((rec[0] - M) * LOG2E);
      L = fmaf(wt, rec[1], L); o0 = fmaf(wt, rec[4 + d], o0); o1 = fmaf(wt, rec[5 + d], o1); }
    if (tid < 256) GSTORE(WS_PA + (g * 32 + h * 8 + ((tid & 63) >> 3)) * 4608 + s * 128 + (tid & 7) * 16, f2u(o0), f2u(o1));
    else           GSTORE(WS_PA + (g * 32 + (tid - 256)) * 4608 + 4096 + s * 16, f2u(M), f2u(L));
  }
  float* const inb = reinterpret_cast<float*>(smem + L_IN);
  if (tid < 256) {
    const unsigned box = WS_PA + (g * 32 + s) * 4608;
    u32x4 a, b;
#pragma unroll 1
    for (int spin = 0; spin < SPIN_MAX; ++spin) {
      asm volatile("" ::: "memory");
      a = GLOAD(box + tid * 16); b = GLOAD(box + 4096 + (lane & 31) * 16);
      if (__all(a[0] == tag && a[2] == tag && b[0] == tag && b[2] == tag)) break;
      __builtin_amdgcn_s_sleep(2);
    }
    const float ms = u2f(b[1]), M = wave_max(ms);
    const float e = __builtin_amdgcn_exp2f((ms - M) * LOG2E);
    const float L = 0.5f * wave_sum(e * u2f(b[3]));
    const float wt = __shfl(e / L, tid >> 3);
    float o0 = wt * u2f(a[1]), o1 = wt * u2f(a[3]);
    o0 += dpp<XOR8>(o0); o1 += dpp<XOR8>(o1);
    o0 = xrow16_sum(o0); o1 = xrow16_sum(o1);
    if (lane < 8) { inb[(2 * lane) * 4 + wave] = o0; inb[(2 * lane + 1) * 4 + wave] = o1; }
  }
  __syncthreads();
  if (tid < 256) {
    float a0 = 0.f, a1 = 0.f;
#pragma unroll
    for (int i = 0; i < 16; ++i) {
      const f32x4 q4 = *reinterpret_cast<const f32x4*>(inb + i * 4);
      const float oi = bfr((q4[0] + q4[1]) + (q4[2] + q4[3]));
      const unsigned w = *reinterpret_cast<const unsigned*>(smem + L_WO + i * 1024 + tid * 4);
      a0 = fmaf(oi, bf2f(w), a0); a1 = fmaf(oi, bfhi(w), a1);
    }
    GSTORE(WS_AP + ((tid >> 2) * 256 + wg) * 64 + (tid & 3) * 16, f2u(a0), f2u(a1));
  }
  const unsigned lbase = (unsigned)(uintptr_t)(LAS unsigned char*)smem;
  if (wg < RB0) {
    const int ju = wg - UP0;
    const unsigned b1v = p.b1[ju * 64 + lane];
    __syncthreads();
    if (wave >= 4) {
      const int w4 = wave - 4;
      const bf16_t* w1p = p.w1 + (size_t)(w4 * 128 + (lane >> 3)) * FF + ju * 64 + (lane & 7) * 8;
#pragma unroll
      for (int i = 0; i < 16; ++i) glds16(w1p + (size_t)i * 8 * FF, lbase + L_W1 + w4 * 16384 + i * 1024);
      const bf16_t* w2p = p.w2 + (size_t)(ju * 64 + w4 * 16) * DM + lane * 8;
#pragma unroll
      for (int i = 0; i < 16; ++i) glds16(w2p + (size_t)i * DM, lbase + L_W2 + w4 * 16384 + i * 1024);
    }
    float* const hs = reinterpret_cast<float*>(smem + L_H);
    if (tid < 256) {
      u32x4 a;
#pragma unroll 1
      for (int spin = 0; spin < SPIN_MAX; ++spin) {
        asm volatile("" ::: "memory");
        a = GLOAD(WS_HG + tid * 16);
        if (__all(a[0] == tag && a[2] == tag)) break;
        __builtin_amdgcn_s_sleep(2);
      }
      hs[2 * tid] = u2f(a[1]); hs[2 * tid + 1] = u2f(a[3]);
    }
    __syncthreads();
    float* const hn = reinterpret_cast<float*>(smem + L_U);
    float* const us = hn + 512;
    {
      float hv[8];
#pragma unroll
      for (int k = 0; k < 8; ++k) hv[k] = hs[lane * 8 + k];
      float mm, dd; ln_stats8(hv, mm, dd);
      hn[tid] = bfr(bfr(hs[tid] - mm) / dd);
    }
    asm volatile("s_waitcnt vmcnt(0)" ::: "memory");
    __syncthreads();
    {
      float y[8] = {0, 0, 0, 0, 0, 0, 0, 0};
#pragma unroll
      for (int i = 0; i < 8; ++i) {
        const int row = (tid >> 3) + 64 * i;
        const u32x4 w = *reinterpret_cast<const u32x4*>(smem + L_W1 + row * 128 + (tid & 7) * 16);
        const float hvv = hn[row];
#pragma unroll
        for (int k = 0; k < 4; ++k) { y[2 * k] = fmaf(hvv, bf2f(w[k]), y[2 * k]); y[2 * k + 1] = fmaf(hvv, bfhi(w[k]), y[2 * k + 1]); }
      }
#pragma unroll
      for (int k = 0; k < 8; ++k) { y[k] += dpp<XOR8>(y[k]); y[k] = xrow16_sum(y[k]); }
      if (lane < 8) {
        *reinterpret_cast<f32x4*>(red + wave * 64 + lane * 8) = f32x4{y[0], y[1], y[2], y[3]};
        *reinterpret_cast<f32x4*>(red + wave * 64 + lane * 8 + 4) = f32x4{y[4], y[5], y[6], y[7]};
      }
    }
    __syncthreads();
    if (wave == 0) {
      float sum = 0.f;
#pragma unroll
      for (int w = 0; w < WAVES; ++w) sum += red[w * 64 + lane];
      const float hv = bfr(bfr(sum) + bf2f(b1v));
      const float rr = hv > 0.f ? hv : 0.f;
      us[lane] = bfr(rr * rr);
    }
    __syncthreads();
    {
      const int cp = tid & 255, half = tid >> 8;
      float a0 = 0.f, a1 = 0.f;
#pragma unroll
      for (int i = 0; i < 8; ++i) {
        const f32x4 u4 = *reinterpret_cast<const f32x4*>(us + half * 32 + i * 4);
#pragma unroll
        for (int k = 0; k < 4; ++k) {
          const unsigned w = *reinterpret_cast<const unsigned*>(smem + L_W2 + (half * 32 + i * 4 + k) * 1024 + cp * 4);
          a0 = fmaf(u4[k], bf2f(w), a0); a1 = fmaf(u4[k], bfhi(w), a1);
        }
      }
      GSTORE(WS_OP + ((cp >> 2) * 56 + ju * 2 + half) * 64 + (cp & 3) * 16, f2u(a0), f2u(a1));
    }
  } else if (wg < RB0 + NRB) {
    const int jb = wg - RB0;
    const unsigned xx = *reinterpret_cast<const unsigned*>(p.x + jb * 8 + 2 * (tid & 3));
    const unsigned bb = *reinterpret_cast<const unsigned*>(p.b2 + jb * 8 + 2 * (tid & 3));
    {
      u32x4 a, b;
#pragma unroll 1
      for (int spin = 0; spin < SPIN_MAX; ++spin) {
        asm volatile("" ::: "memory");
        a = GLOAD(WS_AP + jb * 16384 + tid * 16); b = GLOAD(WS_AP + jb * 16384 + 8192 + tid * 16);
        if (__all(a[0] == tag && a[2] == tag && b[0] == tag && b[2] == tag)) break;
        __builtin_amdgcn_s_sleep(2);
      }
      float s0 = u2f(a[1]) + u2f(b[1]), s1 = u2f(a[3]) + u2f(b[3]);
      s0 += dpp<ROR4>(s0); s0 += dpp<XOR8>(s0); s0 = xrow16_sum(s0);
      s1 += dpp<ROR4>(s1); s1 += dpp<XOR8>(s1); s1 = xrow16_sum(s1);
      if (lane < 4) { red[wave * 8 + 2 * lane] = s0; red[wave * 8 + 2 * lane + 1] = s1; }
    }
    __syncthreads();
    float h0 = 0.f, h1 = 0.f;
    if (tid < 4) {
      float t0 = 0.f, t1 = 0.f;
#pragma unroll
      for (int w = 0; w < WAVES; ++w) { t0 += red[w * 8 + 2 * tid]; t1 += red[w * 8 + 2 * tid + 1]; }
      h0 = bfr(bf2f(xx) + bfr(t0)); h1 = bfr(bfhi(xx) + bfr(t1));
      GSTORE(WS_HG + (jb * 4 + tid) * 16, f2u(h0), f2u(h1));
    }
    if (tid < 256) {
      const unsigned off = WS_OP + jb * (56 * 64) + (tid < 224 ? tid : 0) * 16;
      u32x4 a;
#pragma unroll 1
      for (int spin = 0; spin < SPIN_MAX; ++spin) {
        asm volatile("" ::: "memory");
        a = GLOAD(off);
        if (__all(a[0] == tag && a[2] == tag)) break;
        __builtin_amdgcn_s_sleep(2);
      }
      float s0 = tid < 224 ? u2f(a[1]) : 0.f, s1 = tid < 224 ? u2f(a[3]) : 0.f;
      s0 += dpp<ROR4>(s0); s0 += dpp<XOR8>(s0); s0 = xrow16_sum(s0);
      s1 += dpp<ROR4>(s1); s1 += dpp<XOR8>(s1); s1 = xrow16_sum(s1);
      if (lane < 4) { red[64 + wave * 8 + 2 * lane] = s0; red[64 + wave * 8 + 2 * lane + 1] = s1; }
    }
    __syncthreads();
    if (tid < 4) {
      const float t0 = (red[64 + 2 * tid] + red[72 + 2 * tid]) + (red[80 + 2 * tid] + red[88 + 2 * tid]);
      const float t1 = (red[65 + 2 * tid] + red[73 + 2 * tid]) + (red[81 + 2 * tid] + red[89 + 2 * tid]);
      const float o0 = bfr(bfr(t0) + bf2f(bb)), o1 = bfr(bfr(t1) + bfhi(bb));
      *reinterpret_cast<unsigned*>(p.out + jb * 8 + 2 * tid) = f2bf(h0 + o0) | (f2bf(h1 + o1) << 16);
    }
    if (wg == RB0 && tid == 0) __hip_atomic_store(reinterpret_cast<unsigned*>(p.ws + WS_EPOCH), tag, __ATOMIC_RELAXED, __HIP_MEMORY_SCOPE_AGENT);
  }
}

extern "C" void kernel_launch(void* const* d_in, const int* in_sizes, int n_in,
                              void* d_out, int out_size, void* d_ws, size_t ws_size,
                              hipStream_t stream) {
  Params p;
  p.x  = (const bf16_t*)d_in[0];
  p.kc = (const bf16_t*)d_in[1];
  p.vc = (const bf16_t*)d_in[2];
  p.wq = (const bf16_t*)d_in[3];
  p.wo = (const bf16_t*)d_in[4];
  p.w1 = (const bf16_t*)d_in[6];
  p.b1 = (const bf16_t*)d_in[7];
  p.w2 = (const bf16_t*)d_in[8];
  p.b2 = (const bf16_t*)d_in[9];
  p.out = (bf16_t*)d_out;
  p.ws = (unsigned char*)d_ws;
  fused_decode<<<NWG, NTHR, 0, stream>>>(p);
}
```

```cpp
#include <hip/hip_runtime.h>
#include <hip/hip_bf16.h>
#include <cstdint>

typedef unsigned short bf16_t;
using bf16x2 = __attribute__((ext_vector_type(2))) __bf16;
using u32x4  = __attribute__((ext_vector_type(4))) unsigned;
using u32x2  = __attribute__((ext_vector_type(2))) unsigned;
using f32x4  = __attribute__((ext_vector_type(4))) float;
#define LAS __attribute__((address_space(3)))

constexpr int DM = 512, HQ = 32, HKV = 8, SK = 65536, DH = 128, FF = 1792;
constexpr int NSPLIT = 32, NWG = HKV * NSPLIT, KEYS_PER_WG = SK / NSPLIT, PAGE = 16, WAVES = 8, NTHR = WAVES * 64;
constexpr int PPW = KEYS_PER_WG / PAGE / WAVES;
#define NBUF 3
constexpr int NB = NBUF;
constexpr int UP0 = 0, NUP = FF / 64, RB0 = UP0 + NUP, NRB = DM / 8;
constexpr float SCALE = 0.08838834764831845f;
constexpr float LOG2E = 1.4426950408889634f;
constexpr int SPIN_MAX = 1 << 18;
#define NTW 0

constexpr unsigned WS_EPOCH = 0;
constexpr unsigned WS_QG = 256;
constexpr unsigned WS_PA = WS_QG + 8 * 32 * 4 * 16;
constexpr unsigned WS_AP = WS_PA + 8 * 32 * 32 * 9 * 16;
constexpr unsigned WS_HG = WS_AP + 256 * 256 * 16;
constexpr unsigned WS_OP = WS_HG + 256 * 16;
constexpr unsigned WS_END = WS_OP + 64 * 56 * 4 * 16;

constexpr int L_W1 = 0;
constexpr int L_W2 = 65536;
constexpr int L_WO = L_W2;
constexpr int L_MRG = L_WO + 16384;
constexpr int L_Q = L_W2 + 65536;
constexpr int L_RED = L_Q + 1024;
constexpr int L_H = L_RED + 2048;
constexpr int L_U = L_H + 2048;
constexpr int L_IN = L_U + 2048 + 256;
constexpr int L_END = L_IN + 256;

__device__ __forceinline__ float bf2f(unsigned b) { return __builtin_bit_cast(float, b << 16); }
__device__ __forceinline__ float bfhi(unsigned b) { return __builtin_bit_cast(float, b & 0xffff0000u); }
__device__ __forceinline__ float bfr(float x) { return (float)(__bf16)x; }
__device__ __forceinline__ unsigned f2bf(float x) { return (unsigned)__builtin_bit_cast(unsigned short, (__bf16)x); }
__device__ __forceinline__ float u2f(unsigned u) { return __builtin_bit_cast(float, u); }
__device__ __forceinline__ unsigned f2u(float f) { return __builtin_bit_cast(unsigned, f); }
__device__ __forceinline__ float dot2(unsigned k, unsigned q, float acc) {
  return __builtin_amdgcn_fdot2_f32_bf16(__builtin_bit_cast(bf16x2, k), __builtin_bit_cast(bf16x2, q), acc, false);
}
template <int CTRL> __device__ __forceinline__ float dpp(float x) {
  return __builtin_bit_cast(float, __builtin_amdgcn_mov_dpp(__builtin_bit_cast(int, x), CTRL, 0xf, 0xf, true));
}
constexpr int XOR1 = 0xB1, XOR2 = 0x4E, XOR7 = 0x141, XOR8 = 0x128, ROR4 = 0x124;
__device__ __forceinline__ float xrow16_max(float x) {
  auto s = __builtin_amdgcn_permlane16_swap(__float_as_uint(x), __float_as_uint(x), false, false);
  x = fmaxf(__uint_as_float(s[0]), __uint_as_float(s[1]));
  auto t = __builtin_amdgcn_permlane32_swap(__float_as_uint(x), __float_as_uint(x), false, false);
  return fmaxf(__uint_as_float(t[0]), __uint_as_float(t[1]));
}
__device__ __forceinline__ float xrow16_sum(float x) {
  auto s = __builtin_amdgcn_permlane16_swap(__float_as_uint(x), __float_as_uint(x), false, false);
  x = __uint_as_float(s[0]) + __uint_as_float(s[1]);
  auto t = __builtin_amdgcn_permlane32_swap(__float_as_uint(x), __float_as_uint(x), false, false);
  return __uint_as_float(t[0]) + __uint_as_float(t[1]);
}
__device__ __forceinline__ float wave_sum(float x) {
  x += dpp<XOR1>(x); x += dpp<XOR2>(x); x += dpp<XOR7>(x); x += dpp<XOR8>(x);
  return xrow16_sum(x);
}
__device__ __forceinline__ float wave_max(float x) {
  x = fmaxf(x, dpp<XOR1>(x)); x = fmaxf(x, dpp<XOR2>(x)); x = fmaxf(x, dpp<XOR7>(x)); x = fmaxf(x, dpp<XOR8>(x));
  return xrow16_max(x);
}
__device__ __forceinline__ void ln_stats8(const float (&v)[8], float& m_bf, float& d_bf) {
  float s = 0.f;
#pragma unroll
  for (int k = 0; k < 8; ++k) s += v[k];
  const float mean = wave_sum(s) * (1.f / DM);
  float q = 0.f;
#pragma unroll
  for (int k = 0; k < 8; ++k) { const float d = v[k] - mean; q = fmaf(d, d, q); }
  const float var = bfr(wave_sum(q) * (1.f / DM));
  m_bf = bfr(mean);
  d_bf = bfr(sqrtf(bfr(var + 1e-5f)));
}
__device__ __forceinline__ void glds16(const void* gsrc, unsigned lds_dst) {
  unsigned keep;
  asm volatile("s_mov_b32 %0, m0\n\ts_mov_b32 m0, %2\n\ts_nop 0\n\tglobal_load_lds_dwordx4 %1, off\n\ts_mov_b32 m0, %0"
               : "=&s"(keep) : "v"(gsrc), "s"(lds_dst) : "memory");
}

struct Params { const bf16_t *x, *kc, *vc, *wq, *wo, *w1, *b1, *w2, *b2; bf16_t* out; unsigned char* ws; };

#define STAMP(i_) do { } while (0)
#define GSTORE(off_, d0_, d1_) __builtin_amdgcn_raw_buffer_store_b128(u32x4{tag, (d0_), tag, (d1_)}, rsrc, (off_), 0, 16)
#define GLOAD(off_) __builtin_amdgcn_raw_buffer_load_b128(rsrc, (off_), 0, 16)

__global__ void __launch_bounds__(NTHR, 2) fused_decode(Params p) {
  __shared__ __attribute__((aligned(16))) unsigned char smem[L_END];
  const int wg = blockIdx.x, g = (wg >> 3) & 7, s = (wg & 7) * 4 + (wg >> 6);
  const int tid = threadIdx.x, lane = tid & 63, wave = __builtin_amdgcn_readfirstlane(tid >> 6), r = lane >> 4, c = lane & 15;
  const auto rsrc = __builtin_amdgcn_make_buffer_rsrc(p.ws, 0, WS_END, 0x00020000);
  float* const red = reinterpret_cast<float*>(smem + L_RED);

  if (wave == 0) STAMP(0);
  unsigned ev = __hip_atomic_load(reinterpret_cast<unsigned*>(p.ws + WS_EPOCH), __ATOMIC_RELAXED, __HIP_MEMORY_SCOPE_AGENT);
  u32x4 xv = *reinterpret_cast<const u32x4*>(p.x + 8 * lane);
  unsigned xt = p.x[tid];
  const bf16_t* wqp = p.wq + (size_t)tid * (HQ * DH) + g * 512 + s * 16;
  u32x4 wq0 = *reinterpret_cast<const u32x4*>(wqp), wq1 = *reinterpret_cast<const u32x4*>(wqp + 8);

  __builtin_amdgcn_s_barrier();
  constexpr int SW = WAVES - 1;
  constexpr int NPG = KEYS_PER_WG / PAGE;
  constexpr int PFULL = NPG / SW;
  float* const mrg = reinterpret_cast<float*>(smem + L_MRG);
#define TR_STEP(x, HALF, CTRL, BIT) _Pragma("unroll") for (int hh = 0; hh < (HALF); ++hh) { \
      const float lo_ = x[hh], hi_ = x[hh + (HALF)]; const float t1_ = lo_ + dpp<CTRL>(lo_), t2_ = hi_ + dpp<CTRL>(hi_); \
      x[hh] = (c & (BIT)) ? t2_ : t1_; }
  auto qpartial = [&]() {
    float xf[8];
#pragma unroll
    for (int k = 0; k < 4; ++k) { xf[2 * k] = bf2f(xv[k] & 0xffffu); xf[2 * k + 1] = bfhi(xv[k]); }
    float m, d; ln_stats8(xf, m, d);
    const float xn = bfr(bfr(bf2f(xt) - m) / d);
    float y[16];
#pragma unroll
    for (int k = 0; k < 4; ++k) { y[2 * k] = xn * bf2f(wq0[k] & 0xffffu); y[2 * k + 1] = xn * bfhi(wq0[k]);
                                  y[8 + 2 * k] = xn * bf2f(wq1[k] & 0xffffu); y[9 + 2 * k] = xn * bfhi(wq1[k]); }
    TR_STEP(y, 8, XOR8, 8) TR_STEP(y, 4, XOR7, 4) TR_STEP(y, 2, XOR2, 2) TR_STEP(y, 1, XOR1, 1)
    const float t = xrow16_sum(y[0]);
    if (r == 0) red[wave * 16 + c] = t;
  };
  unsigned tag;
  if (wave == 0) {
    asm volatile("; service wave" : "+v"(ev), "+v"(xv), "+v"(xt), "+v"(wq0), "+v"(wq1) :: "memory");
    qpartial();
    tag = __builtin_amdgcn_readfirstlane(ev) + 1u;
    __syncthreads();
    STAMP(1);
    float qs = 0.f;
#pragma unroll
    for (int w = 0; w < WAVES; ++w) qs += red[w * 16 + c];
    const unsigned qb = f2bf(qs);
    const unsigned b0 = __shfl(qb, 4 * (lane & 3)), b1 = __shfl(qb, 4 * (lane & 3) + 1), b2 = __shfl(qb, 4 * (lane & 3) + 2), b3 = __shfl(qb, 4 * (lane & 3) + 3);
    if (lane < 4) GSTORE(WS_QG + ((g * 32 + s) * 4 + lane) * 16, b0 | (b1 << 16), b2 | (b3 << 16));
    u32x4 a0, a1;
#pragma unroll 1
    for (int spin = 0; spin < SPIN_MAX; ++spin) {
      asm volatile("" ::: "memory");
      a0 = GLOAD(WS_QG + g * 2048 + lane * 16); a1 = GLOAD(WS_QG + g * 2048 + 1024 + lane * 16);
      const bool ok = a0[0] == tag && a0[2] == tag && a1[0] == tag && a1[2] == tag;
      if (__all(ok)) break;
      __builtin_amdgcn_s_sleep(1);
    }
    STAMP(2);
    *reinterpret_cast<u32x2*>(smem + L_Q + lane * 8) = u32x2{a0[1], a0[3]};
    *reinterpret_cast<u32x2*>(smem + L_Q + 512 + lane * 8) = u32x2{a1[1], a1[3]};
    __syncthreads();
    const unsigned lbase = (unsigned)(uintptr_t)(LAS unsigned char*)smem;
    const bf16_t* wop = p.wo + (size_t)(g * 512 + s * 16) * DM + 8 * lane;
#pragma unroll
    for (int i = 0; i < 16; ++i) glds16(wop + i * DM, lbase + L_WO + i * 1024);
    asm volatile("s_waitcnt vmcnt(0)" ::: "memory");
    __syncthreads();
  } else {
    const size_t base = ((size_t)g * SK + (size_t)s * KEYS_PER_WG + (size_t)(wave - 1) * PAGE + r) * DH + 8 * c;
    const bf16_t* kp = p.kc + base;
    const bf16_t* vp = p.vc + base;
    u32x4 Kb[NB][4], Vb[NB][4];
#define ISSUE(b_, i_) do { _Pragma("unroll") for (int t_ = 0; t_ < 4; ++t_) { \
    Kb[b_][t_] = __builtin_nontemporal_load(reinterpret_cast<const u32x4*>(kp + (size_t)(i_) * (SW * PAGE * DH) + t_ * 4 * DH)); \
    Vb[b_][t_] = __builtin_nontemporal_load(reinterpret_cast<const u32x4*>(vp + (size_t)(i_) * (SW * PAGE * DH) + t_ * 4 * DH)); } } while (0)
    ISSUE(0, 0);
    __builtin_amdgcn_sched_barrier(0);
    asm volatile("; streaming waves" : "+v"(ev), "+v"(xv), "+v"(xt), "+v"(wq0), "+v"(wq1) :: "memory");
    qpartial();
    tag = __builtin_amdgcn_readfirstlane(ev) + 1u;
    __syncthreads();
#define HEADMODE 4
    asm volatile("s_waitcnt vmcnt(0)" ::: "memory");
    ISSUE(1, 1);
#pragma unroll
    for (int b = 2; b < NB; ++b) { asm volatile("s_waitcnt vmcnt(0)" ::: "memory"); ISSUE(b, b); }
    __syncthreads();
    u32x4 qpk[4];
#pragma unroll
    for (int h = 0; h < 4; ++h) qpk[h] = *reinterpret_cast<const u32x4*>(smem + L_Q + h * 256 + 16 * c);
    float m = -INFINITY, l = 0.f, acc[32];
#pragma unroll
    for (int k = 0; k < 32; ++k) acc[k] = 0.f;
#define PAGE_BODY(b) do { \
      float x[16]; \
      _Pragma("unroll") for (int t = 0; t < 4; ++t) \
        _Pragma("unroll") for (int h = 0; h < 4; ++h) { float d = 0.f; \
          _Pragma("unroll") for (int k = 0; k < 4; ++k) d = dot2(Kb[b][t][k], qpk[h][k], d); \
          x[t * 4 + h] = d; } \
      TR_STEP(x, 8, XOR8, 8) TR_STEP(x, 4, XOR7, 4) TR_STEP(x, 2, XOR2, 2) TR_STEP(x, 1, XOR1, 1) \
      const float sc = bfr(bfr(x[0]) * SCALE); \
      float pm = sc; pm = fmaxf(pm, dpp<ROR4>(pm)); pm = fmaxf(pm, dpp<XOR8>(pm)); pm = xrow16_max(pm); \
      if (__any(pm > m)) { \
        const float mn = fmaxf(m, pm); \
        const float alpha = __builtin_amdgcn_exp2f((m - mn) * LOG2E); \
        l *= alpha; m = mn; \
        const float a0 = dpp<0x00>(alpha), a1 = dpp<0x55>(alpha), a2 = dpp<0xAA>(alpha), a3 = dpp<0xFF>(alpha); \
        _Pragma("unroll") for (int k = 0; k < 8; ++k) { acc[k] *= a0; acc[8 + k] *= a1; acc[16 + k] *= a2; acc[24 + k] *= a3; } \
      } \
      const float pp = __builtin_amdgcn_exp2f((sc - m) * LOG2E); \
      l += pp; \
      _Pragma("unroll") for (int t = 0; t < 4; ++t) { \
        float vf[8]; \
        _Pragma("unroll") for (int k = 0; k < 4; ++k) { vf[2 * k] = bf2f(Vb[b][t][k]); vf[2 * k + 1] = bfhi(Vb[b][t][k]); } \
        float pb[4]; \
        if (t == 0) { pb[0] = dpp<0x150>(pp); pb[1] = dpp<0x151>(pp); pb[2] = dpp<0x152>(pp); pb[3] = dpp<0x153>(pp); } \
        if (t == 1) { pb[0] = dpp<0x154>(pp); pb[1] = dpp<0x155>(pp); pb[2] = dpp<0x156>(pp); pb[3] = dpp<0x157>(pp); } \
        if (t == 2) { pb[0] = dpp<0x158>(pp); pb[1] = dpp<0x159>(pp); pb[2] = dpp<0x15A>(pp); pb[3] = dpp<0x15B>(pp); } \
        if (t == 3) { pb[0] = dpp<0x15C>(pp); pb[1] = dpp<0x15D>(pp); pb[2] = dpp<0x15E>(pp); pb[3] = dpp<0x15F>(pp); } \
        _Pragma("unroll") for (int h = 0; h < 4; ++h) \
          _Pragma("unroll") for (int k = 0; k < 8; ++k) acc[h * 8 + k] = fmaf(pb[h], vf[k], acc[h * 8 + k]); \
      } } while (0)
    const bool extra = wave <= NPG % SW;
    constexpr int NLOOP = ((PFULL - NB) / NB) * NB;
    for (int i0 = 0; i0 < NLOOP; i0 += NB) {
#pragma unroll
      for (int b = 0; b < NB; ++b) {
        PAGE_BODY(b);
        ISSUE(b, i0 + b + NB);
      }
    }
#pragma unroll
    for (int j = 0; j < PFULL - NLOOP; ++j) {
      PAGE_BODY(j % NB);
      if (NLOOP + j + NB < PFULL || (NLOOP + j + NB == PFULL && extra)) ISSUE(j % NB, NLOOP + j + NB);
    }
    if (extra) PAGE_BODY((PFULL - NLOOP) % NB);
#undef PAGE_BODY
#undef ISSUE
    if (wave == 1) STAMP(3);
    if (wave == 7) STAMP(10);
#pragma unroll
    for (int k = 0; k < 32; ++k) acc[k] = xrow16_sum(acc[k]);
    { float t = l; t += dpp<ROR4>(t); t += dpp<XOR8>(t); l = xrow16_sum(t); }
    if (r == 0) {
#pragma unroll
      for (int h = 0; h < 4; ++h) {
        float* rec = mrg + ((wave - 1) * 4 + h) * 132;
        *reinterpret_cast<f32x4*>(rec + 4 + 8 * c) = f32x4{acc[h * 8], acc[h * 8 + 1], acc[h * 8 + 2], acc[h * 8 + 3]};
        *reinterpret_cast<f32x4*>(rec + 8 + 8 * c) = f32x4{acc[h * 8 + 4], acc[h * 8 + 5], acc[h * 8 + 6], acc[h * 8 + 7]};
        if (c == h) { rec[0] = m; rec[1] = l; }
      }
    }
    __syncthreads();
  }
  if (wave == 0) STAMP(4);
  if (tid < 288) {
    const int h = tid < 256 ? (tid >> 6) : ((tid - 256) >> 3);
    const int d = 2 * (tid & 63);
    float M = -INFINITY;
#pragma unroll
    for (int w = 0; w < SW; ++w) M = fmaxf(M, mrg[(w * 4 + h) * 132]);
    float L = 0.f, o0 = 0.f, o1 = 0.f;
#pragma unroll
    for (int w = 0; w < SW; ++w) { const float* rec = mrg + (w * 4 + h) * 132;
      const float wt = __builtin_amdgcn_exp2f((rec[0] - M) * LOG2E);
      L = fmaf(wt, rec[1], L); o0 = fmaf(wt, rec[4 + d], o0); o1 = fmaf(wt, rec[5 + d], o1); }
    if (tid < 256) GSTORE(WS_PA + (g * 32 + h * 8 + ((tid & 63) >> 3)) * 4608 + s * 128 + (tid & 7) * 16, f2u(o0), f2u(o1));
    else           GSTORE(WS_PA + (g * 32 + (tid - 256)) * 4608 + 4096 + s * 16, f2u(M), f2u(L));
  }
  float* const inb = reinterpret_cast<float*>(smem + L_IN);
  if (tid < 256) {
    const unsigned box = WS_PA + (g * 32 + s) * 4608;
    u32x4 a, b;
#pragma unroll 1
    for (int spin = 0; spin < SPIN_MAX; ++spin) {
      asm volatile("" ::: "memory");
      a = GLOAD(box + tid * 16); b = GLOAD(box + 4096 + (lane & 31) * 16);
      if (__all(a[0] == tag && a[2] == tag && b[0] == tag && b[2] == tag)) break;
      __builtin_amdgcn_s_sleep(2);
    }
    const float ms = u2f(b[1]), M = wave_max(ms);
    const float e = __builtin_amdgcn_exp2f((ms - M) * LOG2E);
    const float L = 0.5f * wave_sum(e * u2f(b[3]));
    const float wt = __shfl(e / L, tid >> 3);
    float o0 = wt * u2f(a[1]), o1 = wt * u2f(a[3]);
    o0 += dpp<XOR8>(o0); o1 += dpp<XOR8>(o1);
    o0 = xrow16_sum(o0); o1 = xrow16_sum(o1);
    if (lane < 8) { inb[(2 * lane) * 4 + wave] = o0; inb[(2 * lane + 1) * 4 + wave] = o1; }
  }
  __syncthreads();
  if (wave == 0) STAMP(5);
  if (tid < 256) {
    float a0 = 0.f, a1 = 0.f;
#pragma unroll
    for (int i = 0; i < 16; ++i) {
      const f32x4 q4 = *reinterpret_cast<const f32x4*>(inb + i * 4);
      const float oi = bfr((q4[0] + q4[1]) + (q4[2] + q4[3]));
      const unsigned w = *reinterpret_cast<const unsigned*>(smem + L_WO + i * 1024 + tid * 4);
      a0 = fmaf(oi, bf2f(w), a0); a1 = fmaf(oi, bfhi(w), a1);
    }
    GSTORE(WS_AP + ((tid >> 2) * 256 + wg) * 64 + (tid & 3) * 16, f2u(a0), f2u(a1));
  }
  const unsigned lbase = (unsigned)(uintptr_t)(LAS unsigned char*)smem;
  if (wg < RB0) {
    const int ju = wg - UP0;
    const unsigned b1v = p.b1[ju * 64 + lane];
    __syncthreads();
    if (wave >= 4) {
      const int w4 = wave - 4;
      const bf16_t* w1p = p.w1 + (size_t)(w4 * 128 + (lane >> 3)) * FF + ju * 64 + (lane & 7) * 8;
#pragma unroll
      for (int i = 0; i < 16; ++i) glds16(w1p + (size_t)i * 8 * FF, lbase + L_W1 + w4 * 16384 + i * 1024);
      const bf16_t* w2p = p.w2 + (size_t)(ju * 64 + w4 * 16) * DM + lane * 8;
#pragma unroll
      for (int i = 0; i < 16; ++i) glds16(w2p + (size_t)i * DM, lbase + L_W2 + w4 * 16384 + i * 1024);
    }
    float* const hs = reinterpret_cast<float*>(smem + L_H);
    if (tid < 256) {
      u32x4 a;
#pragma unroll 1
      for (int spin = 0; spin < SPIN_MAX; ++spin) {
        asm volatile("" ::: "memory");
        a = GLOAD(WS_HG + tid * 16);
        if (__all(a[0] == tag && a[2] == tag)) break;
        __builtin_amdgcn_s_sleep(2);
      }
      hs[2 * tid] = u2f(a[1]); hs[2 * tid + 1] = u2f(a[3]);
    }
    __syncthreads();
    if (wave == 0) STAMP(7);
    float* const hn = reinterpret_cast<float*>(smem + L_U);
    float* const us = hn + 512;
    {
      float hv[8];
#pragma unroll
      for (int k = 0; k < 8; ++k) hv[k] = hs[lane * 8 + k];
      float mm, dd; ln_stats8(hv, mm, dd);
      hn[tid] = bfr(bfr(hs[tid] - mm) / dd);
    }
    asm volatile("s_waitcnt vmcnt(0)" ::: "memory");
    __syncthreads();
    {
      float y[8] = {0, 0, 0, 0, 0, 0, 0, 0};
#pragma unroll
      for (int i = 0; i < 8; ++i) {
        const int row = (tid >> 3) + 64 * i;
        const u32x4 w = *reinterpret_cast<const u32x4*>(smem + L_W1 + row * 128 + (tid & 7) * 16);
        const float hvv = hn[row];
#pragma unroll
        for (int k = 0; k < 4; ++k) { y[2 * k] = fmaf(hvv, bf2f(w[k]), y[2 * k]); y[2 * k + 1] = fmaf(hvv, bfhi(w[k]), y[2 * k + 1]); }
      }
#pragma unroll
      for (int k = 0; k < 8; ++k) { y[k] += dpp<XOR8>(y[k]); y[k] = xrow16_sum(y[k]); }
      if (lane < 8) {
        *reinterpret_cast<f32x4*>(red + wave * 64 + lane * 8) = f32x4{y[0], y[1], y[2], y[3]};
        *reinterpret_cast<f32x4*>(red + wave * 64 + lane * 8 + 4) = f32x4{y[4], y[5], y[6], y[7]};
      }
    }
    __syncthreads();
    if (wave == 0) {
      float sum = 0.f;
#pragma unroll
      for (int w = 0; w < WAVES; ++w) sum += red[w * 64 + lane];
      const float hv = bfr(bfr(sum) + bf2f(b1v));
      const float rr = hv > 0.f ? hv : 0.f;
      us[lane] = bfr(rr * rr);
    }
    __syncthreads();
    {
      const int cp = tid & 255, half = tid >> 8;
      float a0 = 0.f, a1 = 0.f;
#pragma unroll
      for (int i = 0; i < 8; ++i) {
        const f32x4 u4 = *reinterpret_cast<const f32x4*>(us + half * 32 + i * 4);
#pragma unroll
        for (int k = 0; k < 4; ++k) {
          const unsigned w = *reinterpret_cast<const unsigned*>(smem + L_W2 + (half * 32 + i * 4 + k) * 1024 + cp * 4);
          a0 = fmaf(u4[k], bf2f(w), a0); a1 = fmaf(u4[k], bfhi(w), a1);
        }
      }
      GSTORE(WS_OP + ((cp >> 2) * 56 + ju * 2 + half) * 64 + (cp & 3) * 16, f2u(a0), f2u(a1));
    }
  } else if (wg < RB0 + NRB) {
    const int jb = wg - RB0;
    const unsigned xx = *reinterpret_cast<const unsigned*>(p.x + jb * 8 + 2 * (tid & 3));
    const unsigned bb = *reinterpret_cast<const unsigned*>(p.b2 + jb * 8 + 2 * (tid & 3));
    {
      u32x4 a, b;
#pragma unroll 1
      for (int spin = 0; spin < SPIN_MAX; ++spin) {
        asm volatile("" ::: "memory");
        a = GLOAD(WS_AP + jb * 16384 + tid * 16); b = GLOAD(WS_AP + jb * 16384 + 8192 + tid * 16);
        if (__all(a[0] == tag && a[2] == tag && b[0] == tag && b[2] == tag)) break;
        __builtin_amdgcn_s_sleep(2);
      }
      float s0 = u2f(a[1]) + u2f(b[1]), s1 = u2f(a[3]) + u2f(b[3]);
      s0 += dpp<ROR4>(s0); s0 += dpp<XOR8>(s0); s0 = xrow16_sum(s0);
      s1 += dpp<ROR4>(s1); s1 += dpp<XOR8>(s1); s1 = xrow16_sum(s1);
      if (lane < 4) { red[wave * 8 + 2 * lane] = s0; red[wave * 8 + 2 * lane + 1] = s1; }
    }
    __syncthreads();
    if (wave == 0) STAMP(6);
    float h0 = 0.f, h1 = 0.f;
    if (tid < 4) {
      float t0 = 0.f, t1 = 0.f;
#pragma unroll
      for (int w = 0; w < WAVES; ++w) { t0 += red[w * 8 + 2 * tid]; t1 += red[w * 8 + 2 * tid + 1]; }
      h0 = bfr(bf2f(xx) + bfr(t0)); h1 = bfr(bfhi(xx) + bfr(t1));
      GSTORE(WS_HG + (jb * 4 + tid) * 16, f2u(h0), f2u(h1));
    }
    if (tid < 256) {
      const unsigned off = WS_OP + jb * (56 * 64) + (tid < 224 ? tid : 0) * 16;
      u32x4 a;
#pragma unroll 1
      for (int spin = 0; spin < SPIN_MAX; ++spin) {
        asm volatile("" ::: "memory");
        a = GLOAD(off);
        if (__all(a[0] == tag && a[2] == tag)) break;
        __builtin_amdgcn_s_sleep(2);
      }
      float s0 = tid < 224 ? u2f(a[1]) : 0.f, s1 = tid < 224 ? u2f(a[3]) : 0.f;
      s0 += dpp<ROR4>(s0); s0 += dpp<XOR8>(s0); s0 = xrow16_sum(s0);
      s1 += dpp<ROR4>(s1); s1 += dpp<XOR8>(s1); s1 = xrow16_sum(s1);
      if (lane < 4) { red[64 + wave * 8 + 2 * lane] = s0; red[64 + wave * 8 + 2 * lane + 1] = s1; }
    }
    __syncthreads();
    if (wave == 0) STAMP(8);
    if (tid < 4) {
      const float t0 = (red[64 + 2 * tid] + red[72 + 2 * tid]) + (red[80 + 2 * tid] + red[88 + 2 * tid]);
      const float t1 = (red[65 + 2 * tid] + red[73 + 2 * tid]) + (red[81 + 2 * tid] + red[89 + 2 * tid]);
      const float o0 = bfr(bfr(t0) + bf2f(bb)), o1 = bfr(bfr(t1) + bfhi(bb));
      *reinterpret_cast<unsigned*>(p.out + jb * 8 + 2 * tid) = f2bf(h0 + o0) | (f2bf(h1 + o1) << 16);
    }
    if (wave == 0) STAMP(9);
    if (wg == RB0 && tid == 0) __hip_atomic_store(reinterpret_cast<unsigned*>(p.ws + WS_EPOCH), tag, __ATOMIC_RELAXED, __HIP_MEMORY_SCOPE_AGENT);
  }
}

extern "C" void kernel_launch(void* const* d_in, const int* in_sizes, int n_in,
                              void* d_out, int out_size, void* d_ws, size_t ws_size,
                              hipStream_t stream) {
  Params p;
  p.x  = (const bf16_t*)d_in[0];
  p.kc = (const bf16_t*)d_in[1];
  p.vc = (const bf16_t*)d_in[2];
  p.wq = (const bf16_t*)d_in[3];
  p.wo = (const bf16_t*)d_in[4];
  p.w1 = (const bf16_t*)d_in[6];
  p.b1 = (const bf16_t*)d_in[7];
  p.w2 = (const bf16_t*)d_in[8];
  p.b2 = (const bf16_t*)d_in[9];
  p.out = (bf16_t*)d_out;
  p.ws = (unsigned char*)d_ws;
  fused_decode<<<NWG, NTHR, 0, stream>>>(p);
}
```

```cpp
#include <hip/hip_runtime.h>
#include <hip/hip_bf16.h>
#include <cstdint>

typedef unsigned short bf16_t;
using bf16x2 = __attribute__((ext_vector_type(2))) __bf16;
using u32x4  = __attribute__((ext_vector_type(4))) unsigned;
using u32x2  = __attribute__((ext_vector_type(2))) unsigned;
using f32x4  = __attribute__((ext_vector_type(4))) float;
#define LAS __attribute__((address_space(3)))

constexpr int DM = 512, HQ = 32, HKV = 8, SK = 65536, DH = 128, FF = 1792;
constexpr int NSPLIT = 32, NWG = HKV * NSPLIT, KEYS_PER_WG = SK / NSPLIT, PAGE = 16, WAVES = 8, NTHR = WAVES * 64;
constexpr int NB = 3;
constexpr int UP0 = 0, NUP = FF / 64, RB0 = UP0 + NUP, NRB = DM / 8;
constexpr float SCALE = 0.08838834764831845f;
constexpr float LOG2E = 1.4426950408889634f;
constexpr int SPIN_MAX = 1 << 18;

constexpr unsigned WS_EPOCH = 0;
constexpr unsigned WS_QG = 256;
constexpr unsigned WS_PA = WS_QG + 8 * 32 * 4 * 16;
constexpr unsigned WS_AP = WS_PA + 8 * 32 * 32 * 9 * 16;
constexpr unsigned WS_HG = WS_AP + 256 * 256 * 16;
constexpr unsigned WS_OP = WS_HG + 256 * 16;
constexpr unsigned WS_END = WS_OP + 64 * 56 * 4 * 16;

constexpr int L_W1 = 0;
constexpr int L_W2 = 65536;
constexpr int L_WO = L_W2;
constexpr int L_MRG = L_WO + 16384;
constexpr int L_Q = L_W2 + 65536;
constexpr int L_RED = L_Q + 1024;
constexpr int L_H = L_RED + 2048;
constexpr int L_U = L_H + 2048;
constexpr int L_IN = L_U + 2048 + 256;
constexpr int L_END = L_IN + 256;

__device__ __forceinline__ float bf2f(unsigned b) { return __builtin_bit_cast(float, b << 16); }
__device__ __forceinline__ float bfhi(unsigned b) { return __builtin_bit_cast(float, b & 0xffff0000u); }
__device__ __forceinline__ float bfr(float x) { return (float)(__bf16)x; }
__device__ __forceinline__ unsigned f2bf(float x) { return (unsigned)__builtin_bit_cast(unsigned short, (__bf16)x); }
__device__ __forceinline__ float u2f(unsigned u) { return __builtin_bit_cast(float, u); }
__device__ __forceinline__ unsigned f2u(float f) { return __builtin_bit_cast(unsigned, f); }
__device__ __forceinline__ float dot2(unsigned k, unsigned q, float acc) {
  return __builtin_amdgcn_fdot2_f32_bf16(__builtin_bit_cast(bf16x2, k), __builtin_bit_cast(bf16x2, q), acc, false);
}
template <int CTRL> __device__ __forceinline__ float dpp(float x) {
  return __builtin_bit_cast(float, __builtin_amdgcn_mov_dpp(__builtin_bit_cast(int, x), CTRL, 0xf, 0xf, true));
}
constexpr int XOR1 = 0xB1, XOR2 = 0x4E, XOR7 = 0x141, XOR8 = 0x128, ROR4 = 0x124;
__device__ __forceinline__ float xrow16_max(float x) {
  auto s = __builtin_amdgcn_permlane16_swap(__float_as_uint(x), __float_as_uint(x), false, false);
  x = fmaxf(__uint_as_float(s[0]), __uint_as_float(s[1]));
  auto t = __builtin_amdgcn_permlane32_swap(__float_as_uint(x), __float_as_uint(x), false, false);
  return fmaxf(__uint_as_float(t[0]), __uint_as_float(t[1]));
}
__device__ __forceinline__ float xrow16_sum(float x) {
  auto s = __builtin_amdgcn_permlane16_swap(__float_as_uint(x), __float_as_uint(x), false, false);
  x = __uint_as_float(s[0]) + __uint_as_float(s[1]);
  auto t = __builtin_amdgcn_permlane32_swap(__float_as_uint(x), __float_as_uint(x), false, false);
  return __uint_as_float(t[0]) + __uint_as_float(t[1]);
}
__device__ __forceinline__ float wave_sum(float x) {
  x += dpp<XOR1>(x); x += dpp<XOR2>(x); x += dpp<XOR7>(x); x += dpp<XOR8>(x);
  return xrow16_sum(x);
}
__device__ __forceinline__ float wave_max(float x) {
  x = fmaxf(x, dpp<XOR1>(x)); x = fmaxf(x, dpp<XOR2>(x)); x = fmaxf(x, dpp<XOR7>(x)); x = fmaxf(x, dpp<XOR8>(x));
  return xrow16_max(x);
}
__device__ __forceinline__ void ln_stats8(const float (&v)[8], float& m_bf, float& d_bf) {
  float s = 0.f;
#pragma unroll
  for (int k = 0; k < 8; ++k) s += v[k];
  const float mean = wave_sum(s) * (1.f / DM);
  float q = 0.f;
#pragma unroll
  for (int k = 0; k < 8; ++k) { const float d = v[k] - mean; q = fmaf(d, d, q); }
  const float var = bfr(wave_sum(q) * (1.f / DM));
  m_bf = bfr(mean);
  d_bf = bfr(sqrtf(bfr(var + 1e-5f)));
}
__device__ __forceinline__ void glds16(const void* gsrc, unsigned lds_dst) {
  unsigned keep;
  asm volatile("s_mov_b32 %0, m0\n\ts_mov_b32 m0, %2\n\ts_nop 0\n\tglobal_load_lds_dwordx4 %1, off\n\ts_mov_b32 m0, %0"
               : "=&s"(keep) : "v"(gsrc), "s"(lds_dst) : "memory");
}

struct Params { const bf16_t *x, *kc, *vc, *wq, *wo, *w1, *b1, *w2, *b2; bf16_t* out; unsigned char* ws; };

#define GSTORE(off_, d0_, d1_) __builtin_amdgcn_raw_buffer_store_b128(u32x4{tag, (d0_), tag, (d1_)}, rsrc, (off_), 0, 16)
#define GLOAD(off_) __builtin_amdgcn_raw_buffer_load_b128(rsrc, (off_), 0, 16)

__global__ void __launch_bounds__(NTHR, 2) fused_decode(Params p) {
  __shared__ __attribute__((aligned(16))) unsigned char smem[L_END];
  const int wg = blockIdx.x, g = (wg >> 3) & 7, s = (wg & 7) * 4 + (wg >> 6);
  const int tid = threadIdx.x, lane = tid & 63, wave = __builtin_amdgcn_readfirstlane(tid >> 6), r = lane >> 4, c = lane & 15;
  const auto rsrc = __builtin_amdgcn_make_buffer_rsrc(p.ws, 0, WS_END, 0x00020000);
  float* const red = reinterpret_cast<float*>(smem + L_RED);

  unsigned ev = __hip_atomic_load(reinterpret_cast<unsigned*>(p.ws + WS_EPOCH), __ATOMIC_RELAXED, __HIP_MEMORY_SCOPE_AGENT);
  u32x4 xv = *reinterpret_cast<const u32x4*>(p.x + 8 * lane);
  unsigned xt = p.x[tid];
  const bf16_t* wqp = p.wq + (size_t)tid * (HQ * DH) + g * 512 + s * 16;
  u32x4 wq0 = *reinterpret_cast<const u32x4*>(wqp), wq1 = *reinterpret_cast<const u32x4*>(wqp + 8);

  __builtin_amdgcn_s_barrier();
  constexpr int SW = WAVES - 1;
  constexpr int NPG = KEYS_PER_WG / PAGE;
  constexpr int PFULL = NPG / SW;
  float* const mrg = reinterpret_cast<float*>(smem + L_MRG);
#define TR_STEP(x, HALF, CTRL, BIT) _Pragma("unroll") for (int hh = 0; hh < (HALF); ++hh) { \
      const float lo_ = x[hh], hi_ = x[hh + (HALF)]; const float t1_ = lo_ + dpp<CTRL>(lo_), t2_ = hi_ + dpp<CTRL>(hi_); \
      x[hh] = (c & (BIT)) ? t2_ : t1_; }
  auto qpartial = [&]() {
    float xf[8];
#pragma unroll
    for (int k = 0; k < 4; ++k) { xf[2 * k] = bf2f(xv[k] & 0xffffu); xf[2 * k + 1] = bfhi(xv[k]); }
    float m, d; ln_stats8(xf, m, d);
    const float xn = bfr(bfr(bf2f(xt) - m) / d);
    float y[16];
#pragma unroll
    for (int k = 0; k < 4; ++k) { y[2 * k] = xn * bf2f(wq0[k] & 0xffffu); y[2 * k + 1] = xn * bfhi(wq0[k]);
                                  y[8 + 2 * k] = xn * bf2f(wq1[k] & 0xffffu); y[9 + 2 * k] = xn * bfhi(wq1[k]); }
    TR_STEP(y, 8, XOR8, 8) TR_STEP(y, 4, XOR7, 4) TR_STEP(y, 2, XOR2, 2) TR_STEP(y, 1, XOR1, 1)
    const float t = xrow16_sum(y[0]);
    if (r == 0) red[wave * 16 + c] = t;
  };
  unsigned tag;
  if (wave == 0) {
    asm volatile("; service wave" : "+v"(ev), "+v"(xv), "+v"(xt), "+v"(wq0), "+v"(wq1) :: "memory");
    qpartial();
    tag = __builtin_amdgcn_readfirstlane(ev) + 1u;
    __syncthreads();
    float qs = 0.f;
#pragma unroll
    for (int w = 0; w < WAVES; ++w) qs += red[w * 16 + c];
    const unsigned qb = f2bf(qs);
    const unsigned b0 = __shfl(qb, 4 * (lane & 3)), b1 = __shfl(qb, 4 * (lane & 3) + 1), b2 = __shfl(qb, 4 * (lane & 3) + 2), b3 = __shfl(qb, 4 * (lane & 3) + 3);
    if (lane < 4) GSTORE(WS_QG + ((g * 32 + s) * 4 + lane) * 16, b0 | (b1 << 16), b2 | (b3 << 16));
    u32x4 a0, a1;
#pragma unroll 1
    for (int spin = 0; spin < SPIN_MAX; ++spin) {
      asm volatile("" ::: "memory");
      a0 = GLOAD(WS_QG + g * 2048 + lane * 16); a1 = GLOAD(WS_QG + g * 2048 + 1024 + lane * 16);
      const bool ok = a0[0] == tag && a0[2] == tag && a1[0] == tag && a1[2] == tag;
      if (__all(ok)) break;
      __builtin_amdgcn_s_sleep(1);
    }
    *reinterpret_cast<u32x2*>(smem + L_Q + lane * 8) = u32x2{a0[1], a0[3]};
    *reinterpret_cast<u32x2*>(smem + L_Q + 512 + lane * 8) = u32x2{a1[1], a1[3]};
    __syncthreads();
    const unsigned lbase = (unsigned)(uintptr_t)(LAS unsigned char*)smem;
    const bf16_t* wop = p.wo + (size_t)(g * 512 + s * 16) * DM + 8 * lane;
#pragma unroll
    for (int i = 0; i < 16; ++i) glds16(wop + i * DM, lbase + L_WO + i * 1024);
    asm volatile("s_waitcnt vmcnt(0)" ::: "memory");
    __syncthreads();
  } else {
    const size_t base = ((size_t)g * SK + (size_t)s * KEYS_PER_WG + (size_t)(wave - 1) * PAGE + r) * DH + 8 * c;
    const bf16_t* kp = p.kc + base;
    const bf16_t* vp = p.vc + base;
    u32x4 Kb[NB][4], Vb[NB][4];
#define ISSUE(b_, i_) do { _Pragma("unroll") for (int t_ = 0; t_ < 4; ++t_) { \
    Kb[b_][t_] = __builtin_nontemporal_load(reinterpret_cast<const u32x4*>(kp + (size_t)(i_) * (SW * PAGE * DH) + t_ * 4 * DH)); \
    Vb[b_][t_] = __builtin_nontemporal_load(reinterpret_cast<const u32x4*>(vp + (size_t)(i_) * (SW * PAGE * DH) + t_ * 4 * DH)); } } while (0)
    ISSUE(0, 0);
    __builtin_amdgcn_sched_barrier(0);
    asm volatile("; streaming waves" : "+v"(ev), "+v"(xv), "+v"(xt), "+v"(wq0), "+v"(wq1) :: "memory");
    qpartial();
    tag = __builtin_amdgcn_readfirstlane(ev) + 1u;
    __syncthreads();
#pragma unroll
    for (int b = 1; b < NB; ++b) { asm volatile("s_waitcnt vmcnt(0)" ::: "memory"); ISSUE(b, b); }
    __syncthreads();
    u32x4 qpk[4];
#pragma unroll
    for (int h = 0; h < 4; ++h) qpk[h] = *reinterpret_cast<const u32x4*>(smem + L_Q + h * 256 + 16 * c);
    float m = -INFINITY, l = 0.f, acc[32];
#pragma unroll
    for (int k = 0; k < 32; ++k) acc[k] = 0.f;
#define PAGE_BODY(b) do { \
      float x[16]; \
      _Pragma("unroll") for (int t = 0; t < 4; ++t) \
        _Pragma("unroll") for (int h = 0; h < 4; ++h) { float d = 0.f; \
          _Pragma("unroll") for (int k = 0; k < 4; ++k) d = dot2(Kb[b][t][k], qpk[h][k], d); \
          x[t * 4 + h] = d; } \
      TR_STEP(x, 8, XOR8, 8) TR_STEP(x, 4, XOR7, 4) TR_STEP(x, 2, XOR2, 2) TR_STEP(x, 1, XOR1, 1) \
      const float sc = bfr(bfr(x[0]) * SCALE); \
      float pm = sc; pm = fmaxf(pm, dpp<ROR4>(pm)); pm = fmaxf(pm, dpp<XOR8>(pm)); pm = xrow16_max(pm); \
      if (__any(pm > m)) { \
        const float mn = fmaxf(m, pm); \
        const float alpha = __builtin_amdgcn_exp2f((m - mn) * LOG2E); \
        l *= alpha; m = mn; \
        const float a0 = dpp<0x00>(alpha), a1 = dpp<0x55>(alpha), a2 = dpp<0xAA>(alpha), a3 = dpp<0xFF>(alpha); \
        _Pragma("unroll") for (int k = 0; k < 8; ++k) { acc[k] *= a0; acc[8 + k] *= a1; acc[16 + k] *= a2; acc[24 + k] *= a3; } \
      } \
      const float pp = __builtin_amdgcn_exp2f((sc - m) * LOG2E); \
      l += pp; \
      _Pragma("unroll") for (int t = 0; t < 4; ++t) { \
        float vf[8]; \
        _Pragma("unroll") for (int k = 0; k < 4; ++k) { vf[2 * k] = bf2f(Vb[b][t][k]); vf[2 * k + 1] = bfhi(Vb[b][t][k]); } \
        float pb[4]; \
        if (t == 0) { pb[0] = dpp<0x150>(pp); pb[1] = dpp<0x151>(pp); pb[2] = dpp<0x152>(pp); pb[3] = dpp<0x153>(pp); } \
        if (t == 1) { pb[0] = dpp<0x154>(pp); pb[1] = dpp<0x155>(pp); pb[2] = dpp<0x156>(pp); pb[3] = dpp<0x157>(pp); } \
        if (t == 2) { pb[0] = dpp<0x158>(pp); pb[1] = dpp<0x159>(pp); pb[2] = dpp<0x15A>(pp); pb[3] = dpp<0x15B>(pp); } \
        if (t == 3) { pb[0] = dpp<0x15C>(pp); pb[1] = dpp<0x15D>(pp); pb[2] = dpp<0x15E>(pp); pb[3] = dpp<0x15F>(pp); } \
        _Pragma("unroll") for (int h = 0; h < 4; ++h) \
          _Pragma("unroll") for (int k = 0; k < 8; ++k) acc[h * 8 + k] = fmaf(pb[h], vf[k], acc[h * 8 + k]); \
      } } while (0)
    const bool extra = wave <= NPG % SW;
    constexpr int NLOOP = ((PFULL - NB) / NB) * NB;
    for (int i0 = 0; i0 < NLOOP; i0 += NB) {
#pragma unroll
      for (int b = 0; b < NB; ++b) {
        PAGE_BODY(b);
        ISSUE(b, i0 + b + NB);
      }
    }
#pragma unroll
    for (int j = 0; j < PFULL - NLOOP; ++j) {
      PAGE_BODY(j % NB);
      if (NLOOP + j + NB < PFULL || (NLOOP + j + NB == PFULL && extra)) ISSUE(j % NB, NLOOP + j + NB);
    }
    if (extra) PAGE_BODY((PFULL - NLOOP) % NB);
#undef PAGE_BODY
#undef ISSUE
#pragma unroll
    for (int k = 0; k < 32; ++k) acc[k] = xrow16_sum(acc[k]);
    { float t = l; t += dpp<ROR4>(t); t += dpp<XOR8>(t); l = xrow16_sum(t); }
    if (r == 0) {
#pragma unroll
      for (int h = 0; h < 4; ++h) {
        float* rec = mrg + ((wave - 1) * 4 + h) * 132;
        *reinterpret_cast<f32x4*>(rec + 4 + 8 * c) = f32x4{acc[h * 8], acc[h * 8 + 1], acc[h * 8 + 2], acc[h * 8 + 3]};
        *reinterpret_cast<f32x4*>(rec + 8 + 8 * c) = f32x4{acc[h * 8 + 4], acc[h * 8 + 5], acc[h * 8 + 6], acc[h * 8 + 7]};
        if (c == h) { rec[0] = m; rec[1] = l; }
      }
    }
    __syncthreads();
  }
  if (tid < 288) {
    const int h = tid < 256 ? (tid >> 6) : ((tid - 256) >> 3);
    const int d = 2 * (tid & 63);
    float M = -INFINITY;
#pragma unroll
    for (int w = 0; w < SW; ++w) M = fmaxf(M, mrg[(w * 4 + h) * 132]);
    float L = 0.f, o0 = 0.f, o1 = 0.f;
#pragma unroll
    for (int w = 0; w < SW; ++w) { const float* rec = mrg + (w * 4 + h) * 132;
      const float wt = __builtin_amdgcn_exp2f((rec[0] - M) * LOG2E);
      L = fmaf(wt, rec[1], L); o0 = fmaf(wt, rec[4 + d], o0); o1 = fmaf(wt, rec[5 + d], o1); }
    if (tid < 256) GSTORE(WS_PA + (g * 32 + h * 8 + ((tid & 63) >> 3)) * 4608 + s * 128 + (tid & 7) * 16, f2u(o0), f2u(o1));
    else           GSTORE(WS_PA + (g * 32 + (tid - 256)) * 4608 + 4096 + s * 16, f2u(M), f2u(L));
  }
  float* const inb = reinterpret_cast<float*>(smem + L_IN);
  if (tid < 256) {
    const unsigned box = WS_PA + (g * 32 + s) * 4608;
    u32x4 a, b;
#pragma unroll 1
    for (int spin = 0; spin < SPIN_MAX; ++spin) {
      asm volatile("" ::: "memory");
      a = GLOAD(box + tid * 16); b = GLOAD(box + 4096 + (lane & 31) * 16);
      if (__all(a[0] == tag && a[2] == tag && b[0] == tag && b[2] == tag)) break;
      __builtin_amdgcn_s_sleep(2);
    }
    const float ms = u2f(b[1]), M = wave_max(ms);
    const float e = __builtin_amdgcn_exp2f((ms - M) * LOG2E);
    const float L = 0.5f * wave_sum(e * u2f(b[3]));
    const float wt = __shfl(e / L, tid >> 3);
    float o0 = wt * u2f(a[1]), o1 = wt * u2f(a[3]);
    o0 += dpp<XOR8>(o0); o1 += dpp<XOR8>(o1);
    o0 = xrow16_sum(o0); o1 = xrow16_sum(o1);
    if (lane < 8) { inb[(2 * lane) * 4 + wave] = o0; inb[(2 * lane + 1) * 4 + wave] = o1; }
  }
  __syncthreads();
  if (tid < 256) {
    float a0 = 0.f, a1 = 0.f;
#pragma unroll
    for (int i = 0; i < 16; ++i) {
      const f32x4 q4 = *reinterpret_cast<const f32x4*>(inb + i * 4);
      const float oi = bfr((q4[0] + q4[1]) + (q4[2] + q4[3]));
      const unsigned w = *reinterpret_cast<const unsigned*>(smem + L_WO + i * 1024 + tid * 4);
      a0 = fmaf(oi, bf2f(w), a0); a1 = fmaf(oi, bfhi(w), a1);
    }
    GSTORE(WS_AP + ((tid >> 2) * 256 + wg) * 64 + (tid & 3) * 16, f2u(a0), f2u(a1));
  }
  const unsigned lbase = (unsigned)(uintptr_t)(LAS unsigned char*)smem;
  if (wg < RB0) {
    const int ju = wg - UP0;
    const unsigned b1v = p.b1[ju * 64 + lane];
    __syncthreads();
    if (wave >= 4) {
      const int w4 = wave - 4;
      const bf16_t* w1p = p.w1 + (size_t)(w4 * 128 + (lane >> 3)) * FF + ju * 64 + (lane & 7) * 8;
#pragma unroll
      for (int i = 0; i < 16; ++i) glds16(w1p + (size_t)i * 8 * FF, lbase + L_W1 + w4 * 16384 + i * 1024);
      const bf16_t* w2p = p.w2 + (size_t)(ju * 64 + w4 * 16) * DM + lane * 8;
#pragma unroll
      for (int i = 0; i < 16; ++i) glds16(w2p + (size_t)i * DM, lbase + L_W2 + w4 * 16384 + i * 1024);
    }
    float* const hs = reinterpret_cast<float*>(smem + L_H);
    if (tid < 256) {
      u32x4 a;
#pragma unroll 1
      for (int spin = 0; spin < SPIN_MAX; ++spin) {
        asm volatile("" ::: "memory");
        a = GLOAD(WS_HG + tid * 16);
        if (__all(a[0] == tag && a[2] == tag)) break;
        __builtin_amdgcn_s_sleep(2);
      }
      hs[2 * tid] = u2f(a[1]); hs[2 * tid + 1] = u2f(a[3]);
    }
    __syncthreads();
    float* const hn = reinterpret_cast<float*>(smem + L_U);
    float* const us = hn + 512;
    {
      float hv[8];
#pragma unroll
      for (int k = 0; k < 8; ++k) hv[k] = hs[lane * 8 + k];
      float mm, dd; ln_stats8(hv, mm, dd);
      hn[tid] = bfr(bfr(hs[tid] - mm) / dd);
    }
    asm volatile("s_waitcnt vmcnt(0)" ::: "memory");
    __syncthreads();
    {
      float y[8] = {0, 0, 0, 0, 0, 0, 0, 0};
#pragma unroll
      for (int i = 0; i < 8; ++i) {
        const int row = (tid >> 3) + 64 * i;
        const u32x4 w = *reinterpret_cast<const u32x4*>(smem + L_W1 + row * 128 + (tid & 7) * 16);
        const float hvv = hn[row];
#pragma unroll
        for (int k = 0; k < 4; ++k) { y[2 * k] = fmaf(hvv, bf2f(w[k]), y[2 * k]); y[2 * k + 1] = fmaf(hvv, bfhi(w[k]), y[2 * k + 1]); }
      }
#pragma unroll
      for (int k = 0; k < 8; ++k) { y[k] += dpp<XOR8>(y[k]); y[k] = xrow16_sum(y[k]); }
      if (lane < 8) {
        *reinterpret_cast<f32x4*>(red + wave * 64 + lane * 8) = f32x4{y[0], y[1], y[2], y[3]};
        *reinterpret_cast<f32x4*>(red + wave * 64 + lane * 8 + 4) = f32x4{y[4], y[5], y[6], y[7]};
      }
    }
    __syncthreads();
    if (wave == 0) {
      float sum = 0.f;
#pragma unroll
      for (int w = 0; w < WAVES; ++w) sum += red[w * 64 + lane];
      const float hv = bfr(bfr(sum) + bf2f(b1v));
      const float rr = hv > 0.f ? hv : 0.f;
      us[lane] = bfr(rr * rr);
    }
    __syncthreads();
    {
      const int cp = tid & 255, half = tid >> 8;
      float a0 = 0.f, a1 = 0.f;
#pragma unroll
      for (int i = 0; i < 8; ++i) {
        const f32x4 u4 = *reinterpret_cast<const f32x4*>(us + half * 32 + i * 4);
#pragma unroll
        for (int k = 0; k < 4; ++k) {
          const unsigned w = *reinterpret_cast<const unsigned*>(smem + L_W2 + (half * 32 + i * 4 + k) * 1024 + cp * 4);
          a0 = fmaf(u4[k], bf2f(w), a0); a1 = fmaf(u4[k], bfhi(w), a1);
        }
      }
      GSTORE(WS_OP + ((cp >> 2) * 56 + ju * 2 + half) * 64 + (cp & 3) * 16, f2u(a0), f2u(a1));
    }
  } else if (wg < RB0 + NRB) {
    const int jb = wg - RB0;
    const unsigned xx = *reinterpret_cast<const unsigned*>(p.x + jb * 8 + 2 * (tid & 3));
    const unsigned bb = *reinterpret_cast<const unsigned*>(p.b2 + jb * 8 + 2 * (tid & 3));
    {
      u32x4 a, b;
#pragma unroll 1
      for (int spin = 0; spin < SPIN_MAX; ++spin) {
        asm volatile("" ::: "memory");
        a = GLOAD(WS_AP + jb * 16384 + tid * 16); b = GLOAD(WS_AP + jb * 16384 + 8192 + tid * 16);
        if (__all(a[0] == tag && a[2] == tag && b[0] == tag && b[2] == tag)) break;
        __builtin_amdgcn_s_sleep(2);
      }
      float s0 = u2f(a[1]) + u2f(b[1]), s1 = u2f(a[3]) + u2f(b[3]);
      s0 += dpp<ROR4>(s0); s0 += dpp<XOR8>(s0); s0 = xrow16_sum(s0);
      s1 += dpp<ROR4>(s1); s1 += dpp<XOR8>(s1); s1 = xrow16_sum(s1);
      if (lane < 4) { red[wave * 8 + 2 * lane] = s0; red[wave * 8 + 2 * lane + 1] = s1; }
    }
    __syncthreads();
    float h0 = 0.f, h1 = 0.f;
    if (tid < 4) {
      float t0 = 0.f, t1 = 0.f;
#pragma unroll
      for (int w = 0; w < WAVES; ++w) { t0 += red[w * 8 + 2 * tid]; t1 += red[w * 8 + 2 * tid + 1]; }
      h0 = bfr(bf2f(xx) + bfr(t0)); h1 = bfr(bfhi(xx) + bfr(t1));
      GSTORE(WS_HG + (jb * 4 + tid) * 16, f2u(h0), f2u(h1));
    }
    if (tid < 256) {
      const unsigned off = WS_OP + jb * (56 * 64) + (tid < 224 ? tid : 0) * 16;
      u32x4 a;
#pragma unroll 1
      for (int spin = 0; spin < SPIN_MAX; ++spin) {
        asm volatile("" ::: "memory");
        a = GLOAD(off);
        if (__all(a[0] == tag && a[2] == tag)) break;
        __builtin_amdgcn_s_sleep(2);
      }
      float s0 = tid < 224 ? u2f(a[1]) : 0.f, s1 = tid < 224 ? u2f(a[3]) : 0.f;
      s0 += dpp<ROR4>(s0); s0 += dpp<XOR8>(s0); s0 = xrow16_sum(s0);
      s1 += dpp<ROR4>(s1); s1 += dpp<XOR8>(s1); s1 = xrow16_sum(s1);
      if (lane < 4) { red[64 + wave * 8 + 2 * lane] = s0; red[64 + wave * 8 + 2 * lane + 1] = s1; }
    }
    __syncthreads();
    if (tid < 4) {
      const float t0 = (red[64 + 2 * tid] + red[72 + 2 * tid]) + (red[80 + 2 * tid] + red[88 + 2 * tid]);
      const float t1 = (red[65 + 2 * tid] + red[73 + 2 * tid]) + (red[81 + 2 * tid] + red[89 + 2 * tid]);
      const float o0 = bfr(bfr(t0) + bf2f(bb)), o1 = bfr(bfr(t1) + bfhi(bb));
      *reinterpret_cast<unsigned*>(p.out + jb * 8 + 2 * tid) = f2bf(h0 + o0) | (f2bf(h1 + o1) << 16);
    }
    if (wg == RB0 && tid == 0) __hip_atomic_store(reinterpret_cast<unsigned*>(p.ws + WS_EPOCH), tag, __ATOMIC_RELAXED, __HIP_MEMORY_SCOPE_AGENT);
  }
}

extern "C" void kernel_launch(void* const* d_in, const int* in_sizes, int n_in,
                              void* d_out, int out_size, void* d_ws, size_t ws_size,
                              hipStream_t stream) {
  Params p;
  p.x  = (const bf16_t*)d_in[0];
  p.kc = (const bf16_t*)d_in[1];
  p.vc = (const bf16_t*)d_in[2];
  p.wq = (const bf16_t*)d_in[3];
  p.wo = (const bf16_t*)d_in[4];
  p.w1 = (const bf16_t*)d_in[6];
  p.b1 = (const bf16_t*)d_in[7];
  p.w2 = (const bf16_t*)d_in[8];
  p.b2 = (const bf16_t*)d_in[9];
  p.out = (bf16_t*)d_out;
  p.ws = (unsigned char*)d_ws;
  fused_decode<<<NWG, NTHR, 0, stream>>>(p);
}
```
